```python
import torch

def _softmax(x, axis=-1):
    return torch.softmax(x, dim=axis)
def _gelu(x):
    return torch.nn.functional.gelu(x, approximate="tanh")
def _silu(x):
    return torch.nn.functional.silu(x)
def _sigmoid(x):
    return torch.sigmoid(x)
def _layernorm(x, axis=-1, eps=1e-5):
    m = x.mean(dim=axis, keepdim=True)
    v = x.var(dim=axis, keepdim=True, unbiased=False)
    return (x - m) / torch.sqrt(v + eps)
def _scatter_add(tgt, idx, src, axis=0):
    return tgt.clone().index_add_(axis, idx, src)
def _segment_sum(src, seg, n):
    out = torch.zeros((n,) + src.shape[1:], dtype=src.dtype)
    return out.index_add_(0, seg, src)
def _pad(x, pw):
    flat = tuple(v for pair in reversed(pw) for v in pair)
    return torch.nn.functional.pad(x, flat)

kernel_name = 'concat_heads__compressed_kv_cla__gqa_block_cached__decode'

def setup_inputs():

    _w = (lambda a: (lambda t: t.to(torch.bfloat16) if t.is_floating_point() else t)(torch.as_tensor(a)))
    def _A_setup():
        B, H, Sq, Sk, D, M = 2, 32, 1, 8192, 128, 512
        Dc = 4 * D
        return {
            "x": _w((lambda *s: torch.randn(*s, dtype=torch.bfloat16) * 3.2)(B, Sq, M)),
            "c_kv": _w((lambda *s: torch.randn(*s, dtype=torch.bfloat16) * 0.1)(B, Sk, Dc)),
            "w_q": _w((lambda *s: torch.randn(*s, dtype=torch.bfloat16) * 6.4)(M, H, D) * (M ** -0.5)),
            "w_uk": _w((lambda *s: torch.randn(*s, dtype=torch.bfloat16) * 6.4)(Dc, H, D) * (Dc ** -0.5)),
            "w_uv": _w((lambda *s: torch.randn(*s, dtype=torch.bfloat16) * 0.1)(Dc, H, D) * (Dc ** -0.5)),
            "w_o": _w((lambda *s: torch.randn(*s, dtype=torch.bfloat16) * 0.1)(H, D, M) * ((H * D) ** -0.5)),
        }
    _A_in = _A_setup()

    def _B_setup():
        B, Sq, D = 2, 1, 512
        Hq, Hkv, Sk, Dh = 32, 8, 8192, 128
        assert Hq % Hkv == 0, (Hq, Hkv)
        return {
            "x": _w((lambda *s: torch.randn(*s, dtype=torch.bfloat16) * 1.6)(B, Sq, D)),
            "k_cache": _w((lambda *s: torch.randn(*s, dtype=torch.bfloat16) * 1.6)(B, Hkv, Sk, Dh)),
            "v_cache": _w((lambda *s: torch.randn(*s, dtype=torch.bfloat16) * 0.05)(B, Hkv, Sk, Dh)),
            "wq": _w((lambda *s: torch.randn(*s, dtype=torch.bfloat16) * 1.6)(D, Hq * Dh) * (D ** -0.5)),
            "wo": _w((lambda *s: torch.randn(*s, dtype=torch.bfloat16) * 0.05)(Hq * Dh, D) * ((Hq * Dh) ** -0.5)),
        }
    _B_in = _B_setup()

    return {
        "A_x": _A_in["x"],
        "A_c_kv": _A_in["c_kv"],
        "A_w_q": _A_in["w_q"],
        "A_w_uk": _A_in["w_uk"],
        "A_w_uv": _A_in["w_uv"],
        "A_w_o": _A_in["w_o"],
        "B_x": _B_in["x"],
        "B_k_cache": _B_in["k_cache"],
        "B_v_cache": _B_in["v_cache"],
        "B_wq": _B_in["wq"],
        "B_wo": _B_in["wo"],
    }

def reference(A_x, A_c_kv, A_w_q, A_w_uk, A_w_uv, A_w_o, B_x, B_k_cache, B_v_cache, B_wq, B_wo):
    def _A_ref(x, c_kv, w_q, w_uk, w_uv, w_o):
        B, Sq, M = x.shape[0], x.shape[1], x.shape[2]
        Sk = c_kv.shape[1]
        H, D = w_q.shape[1], w_q.shape[2]
        q = torch.einsum("bqm,mhd->bhqd", x, w_q)
        k = torch.einsum("bkc,chd->bhkd", c_kv, w_uk)
        v = torch.einsum("bkc,chd->bhkd", c_kv, w_uv)
        scores = torch.einsum("bhqd,bhkd->bhqk", q, k) * (D ** -0.5)
        off = Sk - Sq
        qi = torch.arange(Sq)[:, None]
        kj = torch.arange(Sk)[None, :]
        cmask = kj <= (qi + off)
        scores = torch.where(cmask[None, None], scores, -1e9)
        p = _softmax(scores, axis=-1)
        ctx = torch.einsum("bhqk,bhkd->bhqd", p, v)
        return torch.einsum("bhqd,hdm->bqm", ctx, w_o)

    def _B_ref(x, k_cache, v_cache, wq, wo):
        B, Sq, D = x.shape
        Hq, Dh = 32, 128
        Hkv = k_cache.shape[1]
        rep = Hq // Hkv
        q = (lambda x, *d: x.permute(*d))((x @ wq).reshape(B, Sq, Hq, Dh), 0, 2, 1, 3)
        k = (lambda x, n, axis: torch.repeat_interleave(x, n, dim=axis))(k_cache, rep, axis=1)
        v = (lambda x, n, axis: torch.repeat_interleave(x, n, dim=axis))(v_cache, rep, axis=1)
        scores = torch.einsum("bhqd,bhkd->bhqk", q, k) * (Dh ** -0.5)
        p = _softmax(scores, axis=-1)
        o = torch.einsum("bhqk,bhkd->bhqd", p, v)
        return (lambda x, *d: x.permute(*d))(o, 0, 2, 1, 3).reshape(B, Sq, Hq * Dh) @ wo

    a = _A_ref(A_x, A_c_kv, A_w_q, A_w_uk, A_w_uv, A_w_o)
    b = _B_ref(B_x, B_k_cache, B_v_cache, B_wq, B_wo)
    return torch.concatenate([a, b], axis=-1)
```

```cpp
#include <hip/hip_runtime.h>
#include <hip/hip_bf16.h>
#include <stdint.h>

typedef unsigned short u16;
__device__ __forceinline__ float bf2f(u16 v) { return __builtin_bit_cast(float, ((unsigned)v) << 16); }
__device__ __forceinline__ u16 f2bf(float f) { unsigned u = __builtin_bit_cast(unsigned, f); u += 0x7FFFu + ((u >> 16) & 1u); return (u16)(u >> 16); }

constexpr int B = 2, H = 32, SK = 8192, D = 128, M = 512, DC = 512, HKV = 8;

__global__ void k_q(const u16* xA, const u16* wqA, const u16* xB, const u16* wqB, float* qA, float* qB) {
  int h = blockIdx.x, br = blockIdx.y, d = threadIdx.x;
  const u16* x = br ? xB : xA; const u16* w = br ? wqB : wqA; float* q = br ? qB : qA;
  float a0 = 0, a1 = 0;
  for (int m = 0; m < M; ++m) { float wv = bf2f(w[(size_t)m * 4096 + h * 128 + d]); a0 += bf2f(x[m]) * wv; a1 += bf2f(x[512 + m]) * wv; }
  q[(0 * H + h) * D + d] = a0; q[(1 * H + h) * D + d] = a1;
}
__global__ void k_qabs(const float* qA, const u16* wuk, float* qabs) {
  int h = blockIdx.x, c = threadIdx.x;
  float a0 = 0, a1 = 0;
  for (int d = 0; d < D; ++d) { float wv = bf2f(wuk[(size_t)c * 4096 + h * 128 + d]); a0 += qA[(0 * H + h) * D + d] * wv; a1 += qA[(1 * H + h) * D + d] * wv; }
  qabs[(0 * H + h) * DC + c] = a0; qabs[(1 * H + h) * DC + c] = a1;
}
__global__ void k_scores(const float* qabs, const u16* ckv, const float* qB, const u16* kc, float* sA, float* sB) {
  int k = blockIdx.x * 256 + threadIdx.x, h = blockIdx.y, b = blockIdx.z;
  float a = 0;
  for (int c = 0; c < DC; ++c) a += qabs[(b * H + h) * DC + c] * bf2f(ckv[((size_t)b * SK + k) * DC + c]);
  sA[((size_t)b * H + h) * SK + k] = a * 0.08838834764831845f;
  a = 0;
  for (int d = 0; d < D; ++d) a += qB[(b * H + h) * D + d] * bf2f(kc[(((size_t)b * HKV + h / 4) * SK + k) * D + d]);
  sB[((size_t)b * H + h) * SK + k] = a * 0.08838834764831845f;
}
__global__ void k_softmax(float* sA, float* sB) {
  __shared__ float red[256];
  float* s = (blockIdx.y ? sB : sA) + (size_t)blockIdx.x * SK;
  int t = threadIdx.x;
  float m = -INFINITY;
  for (int k = t; k < SK; k += 256) m = fmaxf(m, s[k]);
  red[t] = m; __syncthreads();
  for (int o = 128; o > 0; o >>= 1) { if (t < o) red[t] = fmaxf(red[t], red[t + o]); __syncthreads(); }
  m = red[0]; __syncthreads();
  float l = 0;
  for (int k = t; k < SK; k += 256) { float e = expf(s[k] - m); s[k] = e; l += e; }
  red[t] = l; __syncthreads();
  for (int o = 128; o > 0; o >>= 1) { if (t < o) red[t] += red[t + o]; __syncthreads(); }
  l = red[0];
  for (int k = t; k < SK; k += 256) s[k] /= l;
}
__global__ void k_pv(const float* sA, const u16* ckv, const float* sB, const u16* vc, float* latA, float* oB) {
  int h = blockIdx.x, b = blockIdx.y, c = threadIdx.x;
  float a = 0;
  for (int k = 0; k < SK; ++k) a += sA[((size_t)b * H + h) * SK + k] * bf2f(ckv[((size_t)b * SK + k) * DC + c]);
  latA[(b * H + h) * DC + c] = a;
  if (c < D) {
    a = 0;
    for (int k = 0; k < SK; ++k) a += sB[((size_t)b * H + h) * SK + k] * bf2f(vc[(((size_t)b * HKV + h / 4) * SK + k) * D + c]);
    oB[(b * H + h) * D + c] = a;
  }
}
__global__ void k_uv(const float* latA, const u16* wuv, float* oA) {
  int h = blockIdx.x, b = blockIdx.y, d = threadIdx.x;
  float a = 0;
  for (int c = 0; c < DC; ++c) a += latA[(b * H + h) * DC + c] * bf2f(wuv[(size_t)c * 4096 + h * 128 + d]);
  oA[(b * H + h) * D + d] = a;
}
__global__ void k_out(const float* oA, const u16* woA, const float* oB, const u16* woB, u16* out) {
  int b = blockIdx.x, br = blockIdx.y, m = threadIdx.x;
  const float* o = (br ? oB : oA) + b * 4096; const u16* w = br ? woB : woA;
  float a = 0;
  for (int j = 0; j < 4096; ++j) a += o[j] * bf2f(w[(size_t)j * 512 + m]);
  out[b * 1024 + br * 512 + m] = f2bf(a);
}

extern "C" void kernel_launch(void* const* d_in, const int* in_sizes, int n_in, void* d_out, int out_size, void* d_ws, size_t ws_size, hipStream_t stream) {
  const u16* xA = (const u16*)d_in[0]; const u16* ckv = (const u16*)d_in[1]; const u16* wqA = (const u16*)d_in[2];
  const u16* wuk = (const u16*)d_in[3]; const u16* wuv = (const u16*)d_in[4]; const u16* woA = (const u16*)d_in[5];
  const u16* xB = (const u16*)d_in[6]; const u16* kc = (const u16*)d_in[7]; const u16* vc = (const u16*)d_in[8];
  const u16* wqB = (const u16*)d_in[9]; const u16* woB = (const u16*)d_in[10];
  float* ws = (float*)d_ws;
  float* qA = ws; float* qB = qA + B * H * D; float* qabs = qB + B * H * D; float* latA = qabs + B * H * DC;
  float* oA = latA + B * H * DC; float* oB = oA + B * H * D; float* sA = oB + B * H * D; float* sB = sA + (size_t)B * H * SK;
  k_q<<<dim3(32, 2), 128, 0, stream>>>(xA, wqA, xB, wqB, qA, qB);
  k_qabs<<<32, 512, 0, stream>>>(qA, wuk, qabs);
  k_scores<<<dim3(SK / 256, H, B), 256, 0, stream>>>(qabs, ckv, qB, kc, sA, sB);
  k_softmax<<<dim3(64, 2), 256, 0, stream>>>(sA, sB);
  k_pv<<<dim3(H, B), 512, 0, stream>>>(sA, ckv, sB, vc, latA, oB);
  k_uv<<<dim3(H, B), 128, 0, stream>>>(latA, wuv, oA);
  k_out<<<dim3(2, 2), 512, 0, stream>>>(oA, woA, oB, woB, (u16*)d_out);
}
```

```cpp
#include <hip/hip_runtime.h>
#include <hip/hip_bf16.h>
#include <stdint.h>

typedef unsigned short u16;
__device__ __forceinline__ float bf2f(u16 v) { return __builtin_bit_cast(float, ((unsigned)v) << 16); }
__device__ __forceinline__ u16 f2bf(float f) { unsigned u = __builtin_bit_cast(unsigned, f); u += 0x7FFFu + ((u >> 16) & 1u); return (u16)(u >> 16); }

constexpr int B = 2, H = 32, SK = 8192, D = 128, M = 512, DC = 512, HKV = 8;

__global__ void k_q(const u16* xA, const u16* wqA, const u16* xB, const u16* wqB, float* qA, float* qB) {
  int h = blockIdx.x, br = blockIdx.y, d = threadIdx.x;
  const u16* x = br ? xB : xA; const u16* w = br ? wqB : wqA; float* q = br ? qB : qA;
  float a0 = 0, a1 = 0;
  for (int m = 0; m < M; ++m) { float wv = bf2f(w[(size_t)m * 4096 + h * 128 + d]); a0 += bf2f(x[m]) * wv; a1 += bf2f(x[512 + m]) * wv; }
  q[(0 * H + h) * D + d] = a0; q[(1 * H + h) * D + d] = a1;
}
__global__ void k_qabs(const float* qA, const u16* wuk, float* qabs) {
  int h = blockIdx.x, c = threadIdx.x;
  float a0 = 0, a1 = 0;
  for (int d = 0; d < D; ++d) { float wv = bf2f(wuk[(size_t)c * 4096 + h * 128 + d]); a0 += qA[(0 * H + h) * D + d] * wv; a1 += qA[(1 * H + h) * D + d] * wv; }
  qabs[(0 * H + h) * DC + c] = a0; qabs[(1 * H + h) * DC + c] = a1;
}
__global__ void k_scores(const float* qabs, const u16* ckv, const float* qB, const u16* kc, float* sA, float* sB) {
  int k = blockIdx.x * 256 + threadIdx.x, h = blockIdx.y, b = blockIdx.z;
  float a = 0;
  for (int c = 0; c < DC; ++c) a += qabs[(b * H + h) * DC + c] * bf2f(ckv[((size_t)b * SK + k) * DC + c]);
  sA[((size_t)b * H + h) * SK + k] = a * 0.08838834764831845f;
  a = 0;
  for (int d = 0; d < D; ++d) a += qB[(b * H + h) * D + d] * bf2f(kc[(((size_t)b * HKV + h / 4) * SK + k) * D + d]);
  sB[((size_t)b * H + h) * SK + k] = a * 0.08838834764831845f;
}
__global__ void k_softmax(float* sA, float* sB) {
  __shared__ float red[256];
  float* s = (blockIdx.y ? sB : sA) + (size_t)blockIdx.x * SK;
  int t = threadIdx.x;
  float m = -INFINITY;
  for (int k = t; k < SK; k += 256) m = fmaxf(m, s[k]);
  red[t] = m; __syncthreads();
  for (int o = 128; o > 0; o >>= 1) { if (t < o) red[t] = fmaxf(red[t], red[t + o]); __syncthreads(); }
  m = red[0]; __syncthreads();
  float l = 0;
  for (int k = t; k < SK; k += 256) { float e = expf(s[k] - m); s[k] = e; l += e; }
  red[t] = l; __syncthreads();
  for (int o = 128; o > 0; o >>= 1) { if (t < o) red[t] += red[t + o]; __syncthreads(); }
  l = red[0];
  for (int k = t; k < SK; k += 256) s[k] /= l;
}
__global__ void k_pv(const float* sA, const u16* ckv, const float* sB, const u16* vc, float* latA, float* oB) {
  int h = blockIdx.x, b = blockIdx.y, c = threadIdx.x;
  float a = 0;
  for (int k = 0; k < SK; ++k) a += sA[((size_t)b * H + h) * SK + k] * bf2f(ckv[((size_t)b * SK + k) * DC + c]);
  latA[(b * H + h) * DC + c] = a;
  if (c < D) {
    a = 0;
    for (int k = 0; k < SK; ++k) a += sB[((size_t)b * H + h) * SK + k] * bf2f(vc[(((size_t)b * HKV + h / 4) * SK + k) * D + c]);
    oB[(b * H + h) * D + c] = a;
  }
}
__global__ void k_uv(const float* latA, const u16* wuv, float* oA) {
  int h = blockIdx.x, b = blockIdx.y, d = threadIdx.x;
  float a = 0;
  for (int c = 0; c < DC; ++c) a += latA[(b * H + h) * DC + c] * bf2f(wuv[(size_t)c * 4096 + h * 128 + d]);
  oA[(b * H + h) * D + d] = a;
}
__global__ void k_out(const float* oA, const u16* woA, const float* oB, const u16* woB, u16* out) {
  int b = blockIdx.x, br = blockIdx.y, m = threadIdx.x;
  const float* o = (br ? oB : oA) + b * 4096; const u16* w = br ? woB : woA;
  float a = 0;
  for (int j = 0; j < 4096; ++j) a += o[j] * bf2f(w[(size_t)j * 512 + m]);
  out[b * 1024 + br * 512 + m] = f2bf(a);
}


#define GAS __attribute__((address_space(1)))
typedef unsigned v4u __attribute__((ext_vector_type(4)));
struct PArgs { const char* p[9]; };
__global__ void __launch_bounds__(512) k_probe(PArgs a, unsigned long long* stamps) {
  const int lane = threadIdx.x & 63, w = __builtin_amdgcn_readfirstlane(threadIdx.x >> 6);
  unsigned long long t0 = __builtin_amdgcn_s_memrealtime();
  const int gw = blockIdx.x * 8 + w;
  v4u r[52];
  int n = 0;
#pragma unroll
  for (int t = 0; t < 9; ++t) {
    const int cnt = t < 6 ? 2 : (t == 6 ? 8 : 16);
    __amdgpu_buffer_rsrc_t rs = __builtin_amdgcn_make_buffer_rsrc((void*)a.p[t], 0, cnt * 2048 * 1024, 0x00020000);
#pragma unroll
    for (int i = 0; i < cnt; ++i) { r[n] = __builtin_amdgcn_raw_buffer_load_b128(rs, (gw * cnt + i) * 1024 + lane * 16, 0, 0); ++n; }
  }
  __builtin_amdgcn_sched_barrier(0);
  v4u acc = {0, 0, 0, 0};
#pragma unroll
  for (int i = 0; i < 52; ++i) acc ^= r[i];
  unsigned x = acc.x ^ acc.y ^ acc.z ^ acc.w;
  asm volatile("" :: "v"(x));
  asm volatile("s_waitcnt vmcnt(0)" ::: "memory");
  unsigned long long t1 = __builtin_amdgcn_s_memrealtime();
  if (lane == 0) { stamps[gw * 2] = t0; stamps[gw * 2 + 1] = t1; }
}
__global__ void k_delay(const unsigned long long* stamps) {
  unsigned long long mn = ~0ull, mx = 0;
  for (int i = 0; i < 2048; ++i) { unsigned long long a = stamps[2 * i], b = stamps[2 * i + 1]; mn = a < mn ? a : mn; mx = b > mx ? b : mx; }
  unsigned long long span = mx - mn; if (span > 10000) span = 10000;
  unsigned long long t0 = __builtin_amdgcn_s_memrealtime();
  while (__builtin_amdgcn_s_memrealtime() - t0 < span * 0) __builtin_amdgcn_s_sleep(8);
}

extern "C" void kernel_launch(void* const* d_in, const int* in_sizes, int n_in, void* d_out, int out_size, void* d_ws, size_t ws_size, hipStream_t stream) {
  const u16* xA = (const u16*)d_in[0]; const u16* ckv = (const u16*)d_in[1]; const u16* wqA = (const u16*)d_in[2];
  const u16* wuk = (const u16*)d_in[3]; const u16* wuv = (const u16*)d_in[4]; const u16* woA = (const u16*)d_in[5];
  const u16* xB = (const u16*)d_in[6]; const u16* kc = (const u16*)d_in[7]; const u16* vc = (const u16*)d_in[8];
  const u16* wqB = (const u16*)d_in[9]; const u16* woB = (const u16*)d_in[10];
  float* ws = (float*)d_ws;
  float* qA = ws; float* qB = qA + B * H * D; float* qabs = qB + B * H * D; float* latA = qabs + B * H * DC;
  float* oA = latA + B * H * DC; float* oB = oA + B * H * D; float* sA = oB + B * H * D; float* sB = sA + (size_t)B * H * SK;

  { PArgs pa; pa.p[0] = (const char*)wqA; pa.p[1] = (const char*)wuk; pa.p[2] = (const char*)wuv; pa.p[3] = (const char*)woA; pa.p[4] = (const char*)wqB; pa.p[5] = (const char*)woB;
    pa.p[6] = (const char*)ckv; pa.p[7] = (const char*)kc; pa.p[8] = (const char*)vc;
    unsigned long long* stamps = (unsigned long long*)((char*)d_ws + (64 << 20));
    k_probe<<<256, 512, 0, stream>>>(pa, stamps);
    k_delay<<<1, 1, 0, stream>>>(stamps); }
  k_q<<<dim3(32, 2), 128, 0, stream>>>(xA, wqA, xB, wqB, qA, qB);
  k_qabs<<<32, 512, 0, stream>>>(qA, wuk, qabs);
  k_scores<<<dim3(SK / 256, H, B), 256, 0, stream>>>(qabs, ckv, qB, kc, sA, sB);
  k_softmax<<<dim3(64, 2), 256, 0, stream>>>(sA, sB);
  k_pv<<<dim3(H, B), 512, 0, stream>>>(sA, ckv, sB, vc, latA, oB);
  k_uv<<<dim3(H, B), 128, 0, stream>>>(latA, wuv, oA);
  k_out<<<dim3(2, 2), 512, 0, stream>>>(oA, woA, oB, woB, (u16*)d_out);
}
```

```cpp
#include <hip/hip_runtime.h>
#include <hip/hip_bf16.h>
#include <stdint.h>

#define STAMPS 0
#define WIN_B 3
#define WIN_A 8

namespace {

typedef unsigned v4u __attribute__((ext_vector_type(4)));
typedef unsigned v2u __attribute__((ext_vector_type(2)));
typedef float f32x2 __attribute__((ext_vector_type(2)));
typedef float f32x4 __attribute__((ext_vector_type(4)));
typedef float f32x16 __attribute__((ext_vector_type(16)));
typedef __bf16 bf16x2 __attribute__((ext_vector_type(2)));
typedef __bf16 bf16x8 __attribute__((ext_vector_type(8)));
typedef short s16x4 __attribute__((ext_vector_type(4)));
typedef __amdgpu_buffer_rsrc_t rsrc_t;
#define LAS __attribute__((address_space(3)))
#define GAS __attribute__((address_space(1)))

constexpr int AUX_NT = 2, AUX_SC1 = 16;
constexpr float SCL = 0.08838834764831845f * 1.4426950408889634f;
constexpr unsigned SPIN_CAP = 1u << 15;

constexpr int WS_E = 0;
constexpr int WS_G1A = 4096;
constexpr int WS_G1B = WS_G1A + 256 * 1024;
constexpr int WS_QABS = WS_G1B + 256 * 1024;
constexpr int WS_LA = WS_QABS + 128 * 1024;
constexpr int WS_PA = WS_LA + 64 * 1024;
constexpr int WS_CA = WS_PA + 2048 * 1024;
constexpr int WS_OB = WS_CA + 512 * 1024;
constexpr int WS_LB = WS_OB + 384 * 1024;
constexpr int WS_FO = WS_LB + 96 * 1024;
constexpr int WS_END = WS_FO + 512 * 1024;
constexpr int WS_STAMP = 8 << 20;

constexpr int LB_Q = 0;
constexpr int LB_ML = 1024;
constexpr int LB_TAG = 1536;
constexpr int LB_CNT = 1540;
constexpr int LB_O = 2048;
constexpr int LA_MISC = 131072;
constexpr int LA_P = LA_MISC;
constexpr int LA_MX = LA_P + 32 * 144;
constexpr int LA_AL = LA_MX + 512;
constexpr int LA_LS = LA_AL + 128;
constexpr int LA_MF = LA_LS + 512;
constexpr int LA_WT = LA_MF + 128;
constexpr int LA_T1 = 0;
constexpr int LA_T2 = 65536;

struct Params {
  const void* xA; const void* ckv; const void* wqA; const void* wuk; const void* wuv; const void* woA;
  const void* xB; const void* kc; const void* vc; const void* wqB; const void* woB;
  void* out; void* ws;
};

__device__ __forceinline__ rsrc_t mk_rsrc(const void* p, int bytes) { return __builtin_amdgcn_make_buffer_rsrc((void*)p, 0, bytes, 0x00020000); }
template <int AUX> __device__ __forceinline__ v4u ld128(rsrc_t r, int voff, int soff) { return __builtin_amdgcn_raw_buffer_load_b128(r, voff, soff, AUX); }
template <int AUX> __device__ __forceinline__ v2u ld64(rsrc_t r, int voff, int soff) { return __builtin_amdgcn_raw_buffer_load_b64(r, voff, soff, AUX); }
template <int AUX> __device__ __forceinline__ unsigned ld32(rsrc_t r, int voff, int soff) { return __builtin_amdgcn_raw_buffer_load_b32(r, voff, soff, AUX); }
__device__ __forceinline__ void st128(v4u v, rsrc_t r, int voff, int soff) { __builtin_amdgcn_raw_buffer_store_b128(v, r, voff, soff, AUX_SC1); }
__device__ __forceinline__ void st64(v2u v, rsrc_t r, int voff, int soff) { __builtin_amdgcn_raw_buffer_store_b64(v, r, voff, soff, AUX_SC1); }
__device__ __forceinline__ void st32(unsigned v, rsrc_t r, int voff, int soff) { __builtin_amdgcn_raw_buffer_store_b32(v, r, voff, soff, AUX_SC1); }

__device__ __forceinline__ unsigned pk(float lo, float hi) { f32x2 f = {lo, hi}; return __builtin_bit_cast(unsigned, __builtin_convertvector(f, bf16x2)); }
__device__ __forceinline__ float blo(unsigned u) { return __builtin_bit_cast(float, u << 16); }
__device__ __forceinline__ float bhi(unsigned u) { return __builtin_bit_cast(float, u & 0xffff0000u); }
__device__ __forceinline__ float asf(unsigned u) { return __builtin_bit_cast(float, u); }
__device__ __forceinline__ unsigned asu(float f) { return __builtin_bit_cast(unsigned, f); }
__device__ __forceinline__ f32x4 mfma16(v4u a, v4u b, f32x4 c) { return __builtin_amdgcn_mfma_f32_16x16x32_bf16(__builtin_bit_cast(bf16x8, a), __builtin_bit_cast(bf16x8, b), c, 0, 0, 0); }
__device__ __forceinline__ f32x16 mfma32(v4u a, v4u b, f32x16 c) { return __builtin_amdgcn_mfma_f32_32x32x16_bf16(__builtin_bit_cast(bf16x8, a), __builtin_bit_cast(bf16x8, b), c, 0, 0, 0); }
template <int CTRL> __device__ __forceinline__ float dppf(float x) { return asf(__builtin_amdgcn_mov_dpp(asu(x), CTRL, 0xf, 0xf, true)); }
template <int CTRL> __device__ __forceinline__ unsigned dppu(unsigned x) { return __builtin_amdgcn_mov_dpp(x, CTRL, 0xf, 0xf, true); }
constexpr int XOR1 = 0xB1, XOR2 = 0x4E, HMIR = 0x141, ROR8 = 0x128, ROR4 = 0x124;
__device__ __forceinline__ float rows_max(float x) {
  auto s = __builtin_amdgcn_permlane16_swap(asu(x), asu(x), false, false);
  x = fmaxf(asf(s[0]), asf(s[1]));
  auto t = __builtin_amdgcn_permlane32_swap(asu(x), asu(x), false, false);
  return fmaxf(asf(t[0]), asf(t[1]));
}
__device__ __forceinline__ float rows_sum(float x) {
  auto s = __builtin_amdgcn_permlane16_swap(asu(x), asu(x), false, false);
  x = asf(s[0]) + asf(s[1]);
  auto t = __builtin_amdgcn_permlane32_swap(asu(x), asu(x), false, false);
  return asf(t[0]) + asf(t[1]);
}
__device__ __forceinline__ float ex2(float x) { return __builtin_amdgcn_exp2f(x); }
__device__ __forceinline__ float diag_sum(f32x4 d) { return (dppf<0x00>(d[0]) + dppf<0x55>(d[1])) + (dppf<0xAA>(d[2]) + dppf<0xFF>(d[3])); }
template <int J> __device__ __forceinline__ v4u tcol(const v4u* r) {
  constexpr unsigned sel = (J & 1) ? 0x07060302u : 0x05040100u;
  v4u t;
  t[0] = __builtin_amdgcn_perm(r[1][J >> 1], r[0][J >> 1], sel);
  t[1] = __builtin_amdgcn_perm(r[3][J >> 1], r[2][J >> 1], sel);
  t[2] = __builtin_amdgcn_perm(r[5][J >> 1], r[4][J >> 1], sel);
  t[3] = __builtin_amdgcn_perm(r[7][J >> 1], r[6][J >> 1], sel);
  return t;
}
__device__ __forceinline__ void tr8(const v4u* r, v4u* t) {
  t[0] = tcol<0>(r); t[1] = tcol<1>(r); t[2] = tcol<2>(r); t[3] = tcol<3>(r);
  t[4] = tcol<4>(r); t[5] = tcol<5>(r); t[6] = tcol<6>(r); t[7] = tcol<7>(r);
}
__device__ __forceinline__ void gemv8(v4u a, const v4u* r, f32x4* acc) {
  acc[0] = mfma16(a, tcol<0>(r), acc[0]); acc[1] = mfma16(a, tcol<1>(r), acc[1]);
  acc[2] = mfma16(a, tcol<2>(r), acc[2]); acc[3] = mfma16(a, tcol<3>(r), acc[3]);
  acc[4] = mfma16(a, tcol<4>(r), acc[4]); acc[5] = mfma16(a, tcol<5>(r), acc[5]);
  acc[6] = mfma16(a, tcol<6>(r), acc[6]); acc[7] = mfma16(a, tcol<7>(r), acc[7]);
}
__device__ __forceinline__ v4u piece(float a, float b, float c, float d, unsigned tag) { return v4u{pk(a, b), tag, pk(c, d), tag}; }
__device__ __forceinline__ unsigned bad(v4u v, unsigned tag) { return (v[1] ^ tag) | (v[3] ^ tag); }
__device__ __forceinline__ bool all_ok(unsigned badbits) { return __builtin_amdgcn_ballot_w64(badbits != 0u) == 0ull; }

#define SB() __builtin_amdgcn_sched_barrier(0)
#define WAIT_VM(n) asm volatile("s_waitcnt vmcnt(%0)" ::"n"(n) : "memory")
#define BAR() do { SB(); asm volatile("s_waitcnt lgkmcnt(0)\n\ts_barrier" ::: "memory"); SB(); } while (0)
#define KEEP8(a) asm volatile("" : "+v"(a[0]), "+v"(a[1]), "+v"(a[2]), "+v"(a[3]), "+v"(a[4]), "+v"(a[5]), "+v"(a[6]), "+v"(a[7]))
#define ORDER() asm volatile("" ::: "memory")

template <typename T> __device__ __forceinline__ T lds_ld(LAS char* l, int off) { return *(LAS T*)(l + off); }
template <typename T> __device__ __forceinline__ void lds_st(LAS char* l, int off, T v) { *(LAS T*)(l + off) = v; }
__device__ __forceinline__ v2u lds_tr(LAS char* l, int off) { return __builtin_bit_cast(v2u, __builtin_amdgcn_ds_read_tr16_b64_v4i16((LAS s16x4*)(l + off))); }
__device__ __forceinline__ void dma_row(rsrc_t r, int voff, int soff, unsigned ldsaddr) {
  unsigned keep;
  asm volatile("s_mov_b32 %0, m0\n\ts_mov_b32 m0, %4\n\ts_nop 0\n\tbuffer_load_dwordx4 %1, %2, %3 offen nt lds\n\ts_mov_b32 m0, %0"
               : "=&s"(keep) : "v"(voff), "s"(r), "s"(soff), "s"(ldsaddr) : "memory");
}
__device__ __forceinline__ void lds_arrive(LAS char* l, int off, int lane) {
  asm volatile("s_waitcnt lgkmcnt(0)" ::: "memory");
  if (lane == 0) __hip_atomic_fetch_add((LAS unsigned*)(l + off), 1u, __ATOMIC_RELAXED, __HIP_MEMORY_SCOPE_WORKGROUP);
  asm volatile("" ::: "memory");
}
__device__ __forceinline__ void lds_wait(LAS char* l, int off, unsigned want) {
  _Pragma("nounroll") for (unsigned spin = 0; spin < (SPIN_CAP << 6); ++spin) {
    if (__builtin_amdgcn_readfirstlane(*(volatile LAS unsigned*)(l + off)) >= want) break;
  }
  asm volatile("" ::: "memory");
}
__device__ __forceinline__ constexpr int fsw(int row) { return ((row & 3) << 2) | ((row >> 2) & 3); }

#define STAMP(i)
#define STAMP_FLUSH()

__device__ __forceinline__ float qk4(v4u k, v4u q) { return diag_sum(mfma16(k, q, f32x4{0.f, 0.f, 0.f, 0.f})); }

__global__ void __launch_bounds__(512, 2) fused_decode(Params p) {
  __shared__ __attribute__((aligned(16))) char smem[163840];
  LAS char* L = (LAS char*)smem;
  const int lane = threadIdx.x & 63, w = __builtin_amdgcn_readfirstlane(threadIdx.x >> 6);
  const int blk = blockIdx.x, x = blk & 7, y = blk >> 3;
  const int g = lane >> 4, n = lane & 15;
  STAMP(0);
  asm volatile("" : "+s"(p.xA), "+s"(p.ckv), "+s"(p.wqA), "+s"(p.wuk), "+s"(p.wuv), "+s"(p.woA), "+s"(p.xB), "+s"(p.kc), "+s"(p.vc), "+s"(p.wqB), "+s"(p.woB), "+s"(p.out), "+s"(p.ws));
  const rsrc_t ws = mk_rsrc(p.ws, WS_END);
  unsigned tag = 0;
  const int voffV = lane * 16;
  const int voffW8 = g * 8 * 8192 + n * 16;
  const int voffW1 = g * 8 * 1024 + n * 16;

  if (w == 0) {
    const int hq = 4 * x + (y >> 3), mq = y & 7;
    const rsrc_t rxa = mk_rsrc(p.xA, 2048), rxb = mk_rsrc(p.xB, 2048);
    const rsrc_t rwa = mk_rsrc(p.wqA, 4 << 20), rwb = mk_rsrc(p.wqB, 4 << 20), rwk = mk_rsrc(p.wuk, 4 << 20);
    const int xo = (n >> 3) * 1024 + g * 16;
    v4u xa[2], xb[2], wa[16], wb[16];
    const unsigned ep = ld32<AUX_SC1>(ws, 0, WS_E);
    xa[0] = ld128<0>(rxa, xo, mq * 128); xa[1] = ld128<0>(rxa, xo, mq * 128 + 64);
#pragma unroll
    for (int i = 0; i < 16; ++i) wa[i] = ld128<AUX_NT>(rwa, voffW8, (64 * mq + 32 * (i >> 3) + (i & 7)) * 8192 + hq * 256);
    xb[0] = ld128<0>(rxb, xo, mq * 128); xb[1] = ld128<0>(rxb, xo, mq * 128 + 64);
#pragma unroll
    for (int i = 0; i < 16; ++i) wb[i] = ld128<AUX_NT>(rwb, voffW8, (64 * mq + 32 * (i >> 3) + (i & 7)) * 8192 + hq * 256);
    SB();
    STAMP(1);
    tag = __builtin_amdgcn_readfirstlane(ep) + 1u;
    {
      f32x4 acc[8];
#pragma unroll
      for (int j = 0; j < 8; ++j) acc[j] = f32x4{0.f, 0.f, 0.f, 0.f};
      gemv8(xa[0], wa, acc); gemv8(xa[1], wa + 8, acc); KEEP8(acc);
      const bool hi = g & 1;
      st128(piece(hi ? acc[4][0] : acc[0][0], hi ? acc[5][0] : acc[1][0], hi ? acc[6][0] : acc[2][0], hi ? acc[7][0] : acc[3][0], tag), ws, voffV, WS_G1A + (hq * 8 + mq) * 1024);
    }
    SB();
    STAMP(2);
    v4u wk[16];
    const int voffK8 = (n >> 2) * 16 * 8192 + (4 * g + (n & 3)) * 16;
#pragma unroll
    for (int i = 0; i < 16; ++i) wk[i] = ld128<AUX_NT>(rwk, voffK8, (64 * mq + i) * 8192 + hq * 256);
    BAR();
    {
      f32x4 acc[8];
#pragma unroll
      for (int j = 0; j < 8; ++j) acc[j] = f32x4{0.f, 0.f, 0.f, 0.f};
      gemv8(xb[0], wb, acc); gemv8(xb[1], wb + 8, acc); KEEP8(acc);
      const bool hi = g & 1;
      st128(piece(hi ? acc[4][0] : acc[0][0], hi ? acc[5][0] : acc[1][0], hi ? acc[6][0] : acc[2][0], hi ? acc[7][0] : acc[3][0], tag), ws, voffV, WS_G1B + (hq * 8 + mq) * 1024);
    }
    SB();
    STAMP(3);
    v4u qa[8];
    {
      const int go = ((((n >> 2) & 1) * 2 + (n >> 3)) * 16 + 4 * g + (n & 3)) * 16;
      _Pragma("nounroll") for (unsigned spin = 0; spin < SPIN_CAP; ++spin) {
        unsigned bb = 0;
#pragma unroll
        for (int k = 0; k < 8; ++k) qa[k] = ld128<AUX_SC1>(ws, go, WS_G1A + (hq * 8 + k) * 1024);
#pragma unroll
        for (int k = 0; k < 8; ++k) bb |= bad(qa[k], tag);
        if (all_ok(bb)) break;
        ORDER();
      }
    }
    STAMP(4);
    float s0 = 0.f, s1 = 0.f, s2 = 0.f, s3 = 0.f;
#pragma unroll
    for (int k = 0; k < 8; ++k) { s0 += blo(qa[k][0]); s1 += bhi(qa[k][0]); s2 += blo(qa[k][2]); s3 += bhi(qa[k][2]); }
    const unsigned o0 = pk(s0, s1), o1 = pk(s2, s3), p0 = dppu<ROR8>(o0), p1 = dppu<ROR8>(o1);
    const bool up = n >> 3;
    const v4u qf = {up ? p0 : o0, up ? p1 : o1, up ? o0 : p0, up ? o1 : p1};
    float r[16];
#pragma unroll
    for (int i = 0; i < 16; ++i) r[i] = qk4(wk[i], qf);
    const bool d1 = n & 1, d2 = n & 2;
    const unsigned u0 = d2 ? (d1 ? pk(r[12], r[13]) : pk(r[8], r[9])) : (d1 ? pk(r[4], r[5]) : pk(r[0], r[1]));
    const unsigned u1 = d2 ? (d1 ? pk(r[14], r[15]) : pk(r[10], r[11])) : (d1 ? pk(r[6], r[7]) : pk(r[2], r[3]));
    if (n < 8) st128(v4u{u0, tag, u1, tag}, ws, (n >> 2) * 65536 + g * 64 + (n & 3) * 16, WS_QABS + hq * 2048 + mq * 256);
    STAMP(5);
  } else {
    BAR();
    STAMP(1);
  }

  if (y < 24) {
    const int b = y >= 12 ? 1 : 0, s = y - 12 * b;
    const rsrc_t rk = mk_rsrc((const char*)p.kc + (size_t)(b * 8 + x) * (2 << 20), 2 << 20);
    const rsrc_t rv = mk_rsrc((const char*)p.vc + (size_t)(b * 8 + x) * (2 << 20), 2 << 20);
    const int voffK = (n >> 2) * 256 + (4 * g + (n & 3)) * 16;
    if (w != 0) {
      const int g0 = (s * 7 + w - 1) * 1024;
      v4u K[24], V[24];
#pragma unroll
      for (int i = 0; i < 24; ++i) {
        if (i == 1) WAIT_VM(0);
        if (i > WIN_B) WAIT_VM(WIN_B - 1);
        K[i] = ld128<AUX_NT>(rk, voffK, g0 + i * (84 * 1024));
        SB();
      }
#define LDV(i) do { SB(); WAIT_VM(WIN_B - 1); V[i] = ld128<AUX_NT>(rv, voffV, g0 + (i) * (84 * 1024)); SB(); } while (0)
      LDV(0);
      STAMP(2);
      BAR();
      STAMP(3);
      const v4u qf = lds_ld<v4u>(L, LB_Q + lane * 16);
      tag = __builtin_amdgcn_readfirstlane(lds_ld<unsigned>(L, LB_TAG));
      float sc[24];
#pragma unroll
      for (int j = 0; j < 6; ++j) {
#pragma unroll
        for (int i = 4 * j; i < 4 * j + 4; ++i) sc[i] = qk4(K[i], qf);
        LDV(j + 1);
      }
      float m = sc[0];
#pragma unroll
      for (int i = 1; i < 24; ++i) m = fmaxf(m, sc[i]);
      m = rows_max(m) * SCL;
      LDV(7);
      float l = 0.f;
      v4u P[3];
#pragma unroll
      for (int t = 0; t < 3; ++t) {
#pragma unroll
        for (int i = 8 * t; i < 8 * t + 8; ++i) { sc[i] = ex2(__builtin_fmaf(sc[i], SCL, -m)); l += sc[i]; }
        P[t] = v4u{pk(sc[8 * t], sc[8 * t + 1]), pk(sc[8 * t + 2], sc[8 * t + 3]), pk(sc[8 * t + 4], sc[8 * t + 5]), pk(sc[8 * t + 6], sc[8 * t + 7])};
        LDV(8 + t);
      }
      l = rows_sum(l);
      if (lane < 16 && (n & 3) == 0) lds_st<f32x2>(L, LB_ML + w * 32 + (n >> 2) * 8, f32x2{m, l});
      STAMP(4);
      f32x4 acc[8];
#pragma unroll
      for (int j = 0; j < 8; ++j) acc[j] = f32x4{0.f, 0.f, 0.f, 0.f};
#pragma unroll
      for (int i = 11; i < 13; ++i) LDV(i);
      gemv8(P[0], V, acc);
#pragma unroll
      for (int i = 13; i < 21; ++i) LDV(i);
      gemv8(P[1], V + 8, acc);
#pragma unroll
      for (int i = 21; i < 24; ++i) LDV(i);
#undef LDV
      STAMP(5);
      BAR();
      float M = -INFINITY, Ls = 0.f;
      f32x2 ml[8];
#pragma unroll
      for (int v = 0; v < 8; ++v) { ml[v] = lds_ld<f32x2>(L, LB_ML + v * 32 + g * 8); M = fmaxf(M, ml[v][0]); }
#pragma unroll
      for (int v = 0; v < 8; ++v) Ls = __builtin_fmaf(ex2(ml[v][0] - M), ml[v][1], Ls);
      const float mw = lds_ld<float>(L, LB_ML + w * 32 + g * 8);
      const float f = ex2(mw - M) / Ls;
      if (w == 7 && n == 0) st64(v2u{asu(M + __builtin_amdgcn_logf(Ls)), tag}, ws, g * (24 * 128), WS_LB + ((4 * x * 2 + b) * 12 + s) * 128);
      SB();
      gemv8(P[2], V + 16, acc);
      KEEP8(acc);
      lds_st<f32x4>(L, LB_O + w * 2048 + lane * 32, f32x4{acc[0][0] * f, acc[1][0] * f, acc[2][0] * f, acc[3][0] * f});
      lds_st<f32x4>(L, LB_O + w * 2048 + lane * 32 + 16, f32x4{acc[4][0] * f, acc[5][0] * f, acc[6][0] * f, acc[7][0] * f});
      STAMP(6);
      lds_arrive(L, LB_CNT, lane);
      if (w == 1 || w == 2) {
        lds_wait(L, LB_CNT, 8u);
        STAMP(7);
        const int pi = (w - 1) * 64 + lane;
        f32x4 o = lds_ld<f32x4>(L, LB_O + pi * 16);
#pragma unroll
        for (int v = 1; v < 8; ++v) o += lds_ld<f32x4>(L, LB_O + v * 2048 + pi * 16);
        st128(piece(o[0], o[1], o[2], o[3], tag), ws, (lane >> 5) * (24 * 512) + (lane & 31) * 16, WS_OB + (((4 * x + 2 * (w - 1)) * 2 + b) * 12 + s) * 512);
        STAMP(8);
      } else if (s >= 8 && (w == 3 || w == 4)) {
        const int br = w == 3 ? 1 : 0, u = x * 8 + b * 4 + (s - 8);
        v4u f0, f1;
        _Pragma("nounroll") for (unsigned spin = 0; spin < SPIN_CAP; ++spin) {
          f0 = ld128<AUX_SC1>(ws, (lane >> 2) * 128 + (lane & 3) * 16, WS_FO + (br * 64 + u) * 4096);
          f1 = ld128<AUX_SC1>(ws, (lane >> 2) * 128 + (lane & 3) * 16, WS_FO + (br * 64 + u) * 4096 + 2048);
          if (all_ok(bad(f0, tag) | bad(f1, tag))) break;
          ORDER();
        }
        STAMP(8);
        float o[4] = {blo(f0[0]) + blo(f1[0]), bhi(f0[0]) + bhi(f1[0]), blo(f0[2]) + blo(f1[2]), bhi(f0[2]) + bhi(f1[2])};
#pragma unroll
        for (int e = 0; e < 4; ++e) { o[e] += dppf<ROR4>(o[e]); o[e] += dppf<ROR8>(o[e]); o[e] = rows_sum(o[e]); }
        if (lane < 4) {
          const int col = (u >> 5) * 1024 + br * 512 + ((u >> 3) & 3) * 128 + (((u >> 1) & 3) * 4 + lane) * 8 + (u & 1) * 4;
          *(GAS v2u*)((GAS char*)p.out + col * 2) = v2u{pk(o[0], o[1]), pk(o[2], o[3])};
        }
        if (u == 0 && br == 0 && lane == 0) st32(tag, ws, 0, WS_E);
        STAMP(9);
      }
    } else {
      v4u qb[16];
      {
        const int go = (n >> 2) * 8192 + (4 * g + (n & 3)) * 16;
        _Pragma("nounroll") for (unsigned spin = 0; spin < SPIN_CAP; ++spin) {
          unsigned bb = 0;
#pragma unroll
          for (int k = 0; k < 16; ++k) qb[k] = ld128<AUX_SC1>(ws, go, WS_G1B + x * 32768 + (k >> 1) * 1024 + (b * 2 + (k & 1)) * 256);
#pragma unroll
          for (int k = 0; k < 16; ++k) bb |= bad(qb[k], tag);
          if (all_ok(bb)) break;
          ORDER();
        }
      }
      float q8[8];
#pragma unroll
      for (int e = 0; e < 8; ++e) q8[e] = 0.f;
#pragma unroll
      for (int k = 0; k < 16; ++k) { const int e = (k & 1) * 4; q8[e] += blo(qb[k][0]); q8[e + 1] += bhi(qb[k][0]); q8[e + 2] += blo(qb[k][2]); q8[e + 3] += bhi(qb[k][2]); }
      const v4u qf = {pk(q8[0], q8[1]), pk(q8[2], q8[3]), pk(q8[4], q8[5]), pk(q8[6], q8[7])};
      lds_st<v4u>(L, LB_Q + lane * 16, qf);
      if (lane == 0) lds_st<v2u>(L, LB_TAG, v2u{tag, s >= 8 ? 0u : 1u});
      STAMP(6);
      if (s >= 8) {
        const int g0 = (2016 + (s - 8) * 8) * 1024;
        v4u K[8], V[8];
#pragma unroll
        for (int i = 0; i < 8; ++i) K[i] = ld128<AUX_NT>(rk, voffK, g0 + i * 1024);
#pragma unroll
        for (int i = 0; i < 8; ++i) V[i] = ld128<AUX_NT>(rv, voffV, g0 + i * 1024);
        SB();
        BAR();
        float sc[8];
#pragma unroll
        for (int i = 0; i < 8; ++i) sc[i] = qk4(K[i], qf);
        float m = sc[0];
#pragma unroll
        for (int i = 1; i < 8; ++i) m = fmaxf(m, sc[i]);
        m = rows_max(m) * SCL;
        float l = 0.f;
#pragma unroll
        for (int i = 0; i < 8; ++i) { sc[i] = ex2(__builtin_fmaf(sc[i], SCL, -m)); l += sc[i]; }
        const v4u P = {pk(sc[0], sc[1]), pk(sc[2], sc[3]), pk(sc[4], sc[5]), pk(sc[6], sc[7])};
        l = rows_sum(l);
        if (lane < 16 && (n & 3) == 0) lds_st<f32x2>(L, LB_ML + (n >> 2) * 8, f32x2{m, l});
        f32x4 acc[8];
#pragma unroll
        for (int j = 0; j < 8; ++j) acc[j] = f32x4{0.f, 0.f, 0.f, 0.f};
        gemv8(P, V, acc);
        KEEP8(acc);
        BAR();
        float M = -INFINITY, Ls = 0.f;
        f32x2 ml[8];
#pragma unroll
        for (int v = 0; v < 8; ++v) { ml[v] = lds_ld<f32x2>(L, LB_ML + v * 32 + g * 8); M = fmaxf(M, ml[v][0]); }
#pragma unroll
        for (int v = 0; v < 8; ++v) Ls = __builtin_fmaf(ex2(ml[v][0] - M), ml[v][1], Ls);
        const float f = ex2(ml[0][0] - M) / Ls;
        lds_st<f32x4>(L, LB_O + lane * 32, f32x4{acc[0][0] * f, acc[1][0] * f, acc[2][0] * f, acc[3][0] * f});
        lds_st<f32x4>(L, LB_O + lane * 32 + 16, f32x4{acc[4][0] * f, acc[5][0] * f, acc[6][0] * f, acc[7][0] * f});
        lds_arrive(L, LB_CNT, lane);
      } else {
        if (lane < 4) lds_st<f32x2>(L, LB_ML + lane * 8, f32x2{-INFINITY, 0.f});
        lds_st<f32x4>(L, LB_O + lane * 32, f32x4{0.f, 0.f, 0.f, 0.f});
        lds_st<f32x4>(L, LB_O + lane * 32 + 16, f32x4{0.f, 0.f, 0.f, 0.f});
        BAR();
        BAR();
        const int idx = b * 8 + s, h = 4 * x + (idx >> 2), cb = idx & 3;
        const rsrc_t rwo = mk_rsrc(p.woA, 4 << 20);
        STAMP(7);
        v4u ca[16], W[32];
        const int go = ((((n >> 2) & 1) * 2 + (n >> 3)) * 16 + 4 * (n & 3) + g) * 16;
#pragma unroll
        for (int k = 0; k < 16; ++k) ca[k] = ld128<AUX_SC1>(ws, go, WS_CA + (h * 16 + k) * 1024);
#pragma unroll
        for (int i = 0; i < 32; ++i) W[i] = ld128<AUX_NT>(rwo, voffW1, (h * 128 + (i >> 3) * 32 + (i & 7)) * 1024 + cb * 256);
        SB();
        {
          unsigned bb = 0;
#pragma unroll
          for (int k = 0; k < 16; ++k) bb |= bad(ca[k], tag);
          if (!all_ok(bb)) {
            _Pragma("nounroll") for (unsigned spin = 0; spin < SPIN_CAP; ++spin) {
              ORDER();
              bb = 0;
#pragma unroll
              for (int k = 0; k < 16; ++k) ca[k] = ld128<AUX_SC1>(ws, go, WS_CA + (h * 16 + k) * 1024);
#pragma unroll
              for (int k = 0; k < 16; ++k) bb |= bad(ca[k], tag);
              if (all_ok(bb)) break;
            }
          }
        }
        STAMP(8);
        float s0 = 0.f, s1 = 0.f, s2 = 0.f, s3 = 0.f;
#pragma unroll
        for (int k = 0; k < 16; ++k) { s0 += blo(ca[k][0]); s1 += bhi(ca[k][0]); s2 += blo(ca[k][2]); s3 += bhi(ca[k][2]); }
        const unsigned o0 = pk(s0, s1), o1 = pk(s2, s3), p0 = dppu<ROR8>(o0), p1 = dppu<ROR8>(o1);
        const bool up = n >> 3;
        const v4u cf = {up ? p0 : o0, up ? p1 : o1, up ? o0 : p0, up ? o1 : p1};
        f32x4 acc[8];
#pragma unroll
        for (int j = 0; j < 8; ++j) acc[j] = f32x4{0.f, 0.f, 0.f, 0.f};
#pragma unroll
        for (int kq = 0; kq < 4; ++kq) {
          const bool on = (n & 3) == kq;
          gemv8(v4u{on ? cf[0] : 0u, on ? cf[1] : 0u, on ? cf[2] : 0u, on ? cf[3] : 0u}, W + 8 * kq, acc);
        }
        float o[8];
#pragma unroll
        for (int j = 0; j < 8; ++j) o[j] = (acc[j][0] + acc[j][1]) + (acc[j][2] + acc[j][3]);
        const bool hi = g >> 1;
        st128(piece(hi ? o[4] : o[0], hi ? o[5] : o[1], hi ? o[6] : o[2], hi ? o[7] : o[3], tag), ws,
              ((g & 1) * 32 + (n >> 2) * 2 + (g >> 1)) * 4096 + (n & 3) * 16, WS_FO + cb * 8 * 4096 + h * 128);
        STAMP(9);
      }
    }
  } else {
    const int a8 = y - 24, b = x >> 2, sp = (x & 3) * 8 + a8;
    const rsrc_t rc = mk_rsrc((const char*)p.ckv + (size_t)b * (8 << 20), 8 << 20);
    const unsigned lbase = (unsigned)(size_t)L;
    const int hf = lane >> 5, n32 = lane & 31;
    unsigned ep = 0;
    if (w != 0) ep = ld32<AUX_SC1>(ws, 0, WS_E);
    if (w >= 4) {
      const int v = w - 4;
#pragma unroll
      for (int i = 0; i < 32; ++i) {
        if (i >= WIN_A) WAIT_VM(WIN_A - 1);
        dma_row(rc, voffV ^ (((v << 2) | (i & 3)) << 4), ((i >> 4) * 2048 + (i & 15) * 128 + sp * 4 + v) * 1024, lbase + (i >> 4) * 65536 + (4 * (i & 15) + v) * 1024);
      }
      STAMP(2);
    }
    BAR();
    if (w != 0) tag = __builtin_amdgcn_readfirstlane(ep) + 1u;
    {
      v4u qr[8];
      _Pragma("nounroll") for (unsigned spin = 0; spin < SPIN_CAP; ++spin) {
        unsigned bb = 0;
#pragma unroll
        for (int i = 0; i < 8; ++i) qr[i] = ld128<AUX_SC1>(ws, voffV, WS_QABS + b * 65536 + (8 * w + i) * 1024);
#pragma unroll
        for (int i = 0; i < 8; ++i) bb |= bad(qr[i], tag);
        if (all_ok(bb)) break;
        ORDER();
      }
      STAMP(6);
#pragma unroll
      for (int i = 0; i < 8; ++i)
        lds_st<v2u>(L, LA_MISC + (4 * w + (i >> 1)) * 1024 + (i & 1) * 512 + ((lane * 8) ^ ((((i >> 1) << 2) | (w & 3)) << 4)), v2u{qr[i][0], qr[i][2]});
    }
    BAR();
    STAMP(7);
    v4u WU[8];
    const rsrc_t rwu = mk_rsrc(p.wuv, 4 << 20);
#define LDU(i) WU[i] = ld128<AUX_NT>(rwu, voffW8, (64 * a8 + 32 * (w >> 2) + (i)) * 8192 + (4 * x + (w & 3)) * 256)
#define DMA(r, i) dma_row(rc, voffV ^ ((((w & 3) << 2) | ((2 * (i) + (w >> 2)) & 3)) << 4), ((r) * 2048 + (2 * (i) + (w >> 2)) * 128 + sp * 4 + (w & 3)) * 1024, lbase + ((r) & 1) * 65536 + (8 * (i) + w) * 1024)
    LDU(0); LDU(1); LDU(2);
    SB();
    const int t = w & 3, hh = w >> 2;
    const int fn = (((n & 3) << 2) | ((n >> 2) & 3)) << 4;
    v4u qf[16];
#pragma unroll
    for (int ks = 0; ks < 16; ++ks) qf[ks] = lds_ld<v4u>(L, LA_MISC + (((16 * hh + n) * 1024 + g * 256 + fn) ^ (ks << 4)));
    const int qkbase = (16 * t + n) * 1024 + g * 256 + fn;
    int trb[4];
    {
      const int blk2 = (lane >> 4) & 1, q = (lane & 15) >> 2, pp = lane & 3;
#pragma unroll
      for (int e = 0; e < 4; ++e) {
        const int ct = e >> 1, tt = e & 1;
        const int chunk = 8 * w + 4 * ct + 2 * blk2 + (pp >> 1);
        trb[e] = (8 * hf + 4 * tt + q) * 1024 + (chunk >> 4) * 256 + (((chunk & 15) ^ ((q << 2) | (2 * hf + tt))) << 4) + 8 * (pp & 1);
      }
    }
    f32x16 acc0, acc1;
#pragma unroll
    for (int e = 0; e < 16; ++e) { acc0[e] = 0.f; acc1[e] = 0.f; }
    float mrun = -INFINITY, lrun = 0.f;
#pragma unroll
    for (int r = 0; r < 4; ++r) {
      const int bo = (r & 1) * 65536;
      if (r == 2) WAIT_VM(8); else if (r == 3) WAIT_VM(0);
      BAR();
      if (r == 0) STAMP(8);
      if (r == 2) STAMP(9);
      if (r == 3) STAMP(10);
      if (r == 1) { DMA(2, 0); DMA(2, 1); DMA(2, 2); SB(); }
      f32x4 sa = {0.f, 0.f, 0.f, 0.f};
      {
        v4u cf[8];
#pragma unroll
        for (int ks = 0; ks < 8; ++ks) cf[ks] = lds_ld<v4u>(L, bo + (qkbase ^ (ks << 4)));
        SB();
#pragma unroll
        for (int ks = 0; ks < 16; ++ks) {
          sa = mfma16(cf[ks & 7], qf[ks], sa);
          if (ks < 8) cf[ks] = lds_ld<v4u>(L, bo + (qkbase ^ ((ks + 8) << 4)));
          SB();
        }
      }
      if (r == 0) { LDU(3); LDU(4); LDU(5); SB(); }
      if (r == 1) { DMA(2, 3); DMA(2, 4); DMA(2, 5); SB(); }
      const float mx = rows_max(fmaxf(fmaxf(sa[0], sa[1]), fmaxf(sa[2], sa[3])));
      if (g == 0) lds_st<float>(L, LA_MX + (16 * hh + n) * 16 + t * 4, mx);
      BAR();
      const f32x4 m4 = lds_ld<f32x4>(L, LA_MX + (16 * hh + n) * 16);
      const float mnew = fmaxf(mrun, fmaxf(fmaxf(m4[0], m4[1]), fmaxf(m4[2], m4[3])) * SCL);
      const float al = ex2(mrun - mnew);
      mrun = mnew;
      const float p0 = ex2(__builtin_fmaf(sa[0], SCL, -mnew)), p1 = ex2(__builtin_fmaf(sa[1], SCL, -mnew));
      const float p2 = ex2(__builtin_fmaf(sa[2], SCL, -mnew)), p3 = ex2(__builtin_fmaf(sa[3], SCL, -mnew));
      lrun = __builtin_fmaf(lrun, al, (p0 + p1) + (p2 + p3));
      lds_st<v2u>(L, LA_P + (16 * hh + n) * 144 + (16 * t + 4 * g) * 2, v2u{pk(p0, p1), pk(p2, p3)});
      if (r > 0 && t == 0 && g == 0) lds_st<float>(L, LA_AL + (16 * hh + n) * 4, al);
      if (r == 3) {
        const float ls = rows_sum(lrun);
        if (g == 0) lds_st<float>(L, LA_LS + (16 * hh + n) * 16 + t * 4, ls);
        if (t == 0 && g == 0) lds_st<float>(L, LA_MF + (16 * hh + n) * 4, mrun);
      }
      if (r == 0) { LDU(6); LDU(7); }
      if (r == 1) { DMA(2, 6); DMA(2, 7); }
      BAR();
      if (r > 0) {
#pragma unroll
        for (int q = 0; q < 4; ++q) {
          const f32x4 a4 = lds_ld<f32x4>(L, LA_AL + (8 * q + 4 * hf) * 4);
#pragma unroll
          for (int e = 0; e < 4; ++e) { acc0[4 * q + e] *= a4[e]; acc1[4 * q + e] *= a4[e]; }
        }
      }
#pragma unroll
      for (int ks = 0; ks < 4; ++ks) {
        const v4u pf = lds_ld<v4u>(L, LA_P + n32 * 144 + (16 * ks + 8 * hf) * 2);
        const v2u c00 = lds_tr(L, bo + trb[0] + ks * 16384), c01 = lds_tr(L, bo + trb[1] + ks * 16384);
        const v2u c10 = lds_tr(L, bo + trb[2] + ks * 16384), c11 = lds_tr(L, bo + trb[3] + ks * 16384);
        acc0 = mfma32(pf, v4u{c00[0], c00[1], c01[0], c01[1]}, acc0);
        acc1 = mfma32(pf, v4u{c10[0], c10[1], c11[0], c11[1]}, acc1);
      }
      if (r == 1) {
        BAR();
#pragma unroll
        for (int i = 0; i < 8; ++i) DMA(3, i);
      }
    }
    STAMP(11);
#pragma unroll
    for (int q = 0; q < 4; ++q)
      st128(v4u{pk(acc0[4 * q], acc0[4 * q + 1]), pk(acc0[4 * q + 2], acc0[4 * q + 3]), pk(acc1[4 * q], acc1[4 * q + 1]), pk(acc1[4 * q + 2], acc1[4 * q + 3])},
            ws, hf * (8 * 32768) + n32 * 16, WS_PA + ((2 * q * 8 + w) * 64 + b * 32 + sp) * 512);
    v4u mlp;
    if (w == 0) {
      const f32x2 mm = lds_ld<f32x2>(L, LA_MF + n * 8);
      const f32x4 l0 = lds_ld<f32x4>(L, LA_LS + n * 32), l1 = lds_ld<f32x4>(L, LA_LS + n * 32 + 16);
      const float la = (l0[0] + l0[1]) + (l0[2] + l0[3]), lb = (l1[0] + l1[1]) + (l1[2] + l1[3]);
      mlp = v4u{asu(g ? la : mm[0]), tag, asu(g ? lb : mm[1]), tag};
    }
    v4u TU[8];
    tr8(WU, TU);
    KEEP8(TU);
    WAIT_VM(0);
    BAR();
    if (w == 0 && lane < 32) st128(mlp, ws, (n >> 1) * 8192 + g * 32 + (n & 1) * 16, WS_LA + b * 4096 + sp * 128);
    STAMP(12);
    {
      const int bb_ = w >> 2;
      v4u mv, lv;
      _Pragma("nounroll") for (unsigned spin = 0; spin < SPIN_CAP; ++spin) {
        mv = ld128<AUX_SC1>(ws, (lane >> 1) * 128 + (lane & 1) * 16, WS_LA + x * 8192 + bb_ * 4096);
        lv = ld128<AUX_SC1>(ws, (lane >> 1) * 128 + (lane & 1) * 16, WS_LA + x * 8192 + bb_ * 4096 + 32);
        if (all_ok(bad(mv, tag) | bad(lv, tag))) break;
        ORDER();
      }
      ORDER();
      v4u pa[4];
#pragma unroll
      for (int i = 0; i < 4; ++i) pa[i] = ld128<AUX_SC1>(ws, voffV, WS_PA + ((x * 8 + a8) * 64 + w * 8 + 2 * i) * 512);
      SB();
      STAMP(13);
      float wt[2];
#pragma unroll
      for (int e = 0; e < 2; ++e) {
        const float ms = asf(mv[2 * e]), ls = asf(lv[2 * e]);
        float M = fmaxf(ms, dppf<XOR2>(ms)); M = fmaxf(M, dppf<ROR4>(M)); M = fmaxf(M, dppf<ROR8>(M)); M = rows_max(M);
        const float ee = ex2(ms - M);
        float S = ee * ls; S += dppf<XOR2>(S); S += dppf<ROR4>(S); S += dppf<ROR8>(S); S = rows_sum(S);
        wt[e] = ee / S;
      }
      lds_st<f32x2>(L, LA_WT + w * 512 + lane * 8, f32x2{wt[0], wt[1]});
      float c8[8];
#pragma unroll
      for (int e = 0; e < 8; ++e) c8[e] = 0.f;
#pragma unroll
      for (int i = 0; i < 4; ++i) {
        const f32x4 w4 = lds_ld<f32x4>(L, LA_WT + w * 512 + (8 * (w & 3) + 2 * i + hf) * 16);
        c8[0] = __builtin_fmaf(w4[0], blo(pa[i][0]), c8[0]); c8[1] = __builtin_fmaf(w4[1], bhi(pa[i][0]), c8[1]);
        c8[2] = __builtin_fmaf(w4[2], blo(pa[i][1]), c8[2]); c8[3] = __builtin_fmaf(w4[3], bhi(pa[i][1]), c8[3]);
        c8[4] = __builtin_fmaf(w4[0], blo(pa[i][2]), c8[4]); c8[5] = __builtin_fmaf(w4[1], bhi(pa[i][2]), c8[5]);
        c8[6] = __builtin_fmaf(w4[2], blo(pa[i][3]), c8[6]); c8[7] = __builtin_fmaf(w4[3], bhi(pa[i][3]), c8[7]);
      }
#pragma unroll
      for (int e = 0; e < 8; ++e) lds_st<float>(L, LA_T1 + (2 * w + hf) * 1040 + e * 128 + n32 * 4, c8[e]);
    }
    const int hB = 4 * x + (a8 >> 1), cbB = 2 * (a8 & 1) + (w >> 2);
    BAR();
    {
      const f32x4 u0 = lds_ld<f32x4>(L, LA_T1 + n * 1040 + w * 128 + g * 32), u1 = lds_ld<f32x4>(L, LA_T1 + n * 1040 + w * 128 + g * 32 + 16);
      float c8[8] = {u0[0], u0[1], u0[2], u0[3], u1[0], u1[1], u1[2], u1[3]};
#pragma unroll
      for (int e = 0; e < 8; ++e) { c8[e] += dppf<XOR1>(c8[e]); c8[e] += dppf<XOR2>(c8[e]); c8[e] += dppf<HMIR>(c8[e]); }
      const v4u af = {pk(c8[0], c8[1]), pk(c8[2], c8[3]), pk(c8[4], c8[5]), pk(c8[6], c8[7])};
      f32x4 d[8];
      float o[8];
#pragma unroll
      for (int j = 0; j < 8; ++j) d[j] = mfma16(af, TU[j], f32x4{0.f, 0.f, 0.f, 0.f});
      KEEP8(d);
#pragma unroll
      for (int j = 0; j < 8; ++j) o[j] = d[j][0];
      const bool hi = g & 1;
      st128(piece(hi ? o[4] : o[0], hi ? o[5] : o[1], hi ? o[6] : o[2], hi ? o[7] : o[3], tag), ws, voffV, WS_CA + ((4 * x + (w & 3)) * 16 + a8 * 2 + (w >> 2)) * 1024);
    }
    STAMP(14);
    v4u WO[8];
    {
      const rsrc_t rwo = mk_rsrc(p.woB, 4 << 20);
#pragma unroll
      for (int i = 0; i < 8; ++i) WO[i] = ld128<AUX_NT>(rwo, voffW1, (hB * 128 + 32 * (w & 3) + i) * 1024 + cbB * 256);
      SB();
    }
    {
      v4u TO[8];
      tr8(WO, TO);
      KEEP8(TO);
      const int s1 = n & 7, s2 = s1 < 4 ? s1 + 8 : s1;
      const int o1 = ((n >> 3) * 12 + s1) * 512 + g * 32, o2 = ((n >> 3) * 12 + s2) * 512 + g * 32;
      const int ob = WS_OB + hB * (24 * 512) + (w & 3) * 128;
      v4u a0, a1, b0, b1; v2u e1, e2;
      _Pragma("nounroll") for (unsigned spin = 0; spin < SPIN_CAP; ++spin) {
        e1 = ld64<AUX_SC1>(ws, ((n >> 3) * 12 + s1) * 128, WS_LB + hB * (24 * 128));
        e2 = ld64<AUX_SC1>(ws, ((n >> 3) * 12 + s2) * 128, WS_LB + hB * (24 * 128));
        if (all_ok((e1[1] ^ tag) | (e2[1] ^ tag))) break;
        __builtin_amdgcn_s_sleep(2);
        ORDER();
      }
      const float l1 = asf(e1[0]), l2 = asf(e2[0]);
      float M = fmaxf(l1, l2); M = fmaxf(M, dppf<XOR1>(M)); M = fmaxf(M, dppf<XOR2>(M)); M = fmaxf(M, dppf<HMIR>(M));
      float w1 = ex2(l1 - M), w2 = s1 < 4 ? ex2(l2 - M) : 0.f;
      float S = w1 + w2; S += dppf<XOR1>(S); S += dppf<XOR2>(S); S += dppf<HMIR>(S);
      const float inv = __builtin_amdgcn_rcpf(S);
      w1 *= inv; w2 *= inv;
      asm volatile("" : "+v"(w1), "+v"(w2));
      _Pragma("nounroll") for (unsigned spin = 0; spin < SPIN_CAP; ++spin) {
        a0 = ld128<AUX_SC1>(ws, o1, ob); a1 = ld128<AUX_SC1>(ws, o1, ob + 16);
        b0 = ld128<AUX_SC1>(ws, o2, ob); b1 = ld128<AUX_SC1>(ws, o2, ob + 16);
        if (all_ok(bad(a0, tag) | bad(a1, tag) | bad(b0, tag) | bad(b1, tag))) break;
        ORDER();
      }
      STAMP(15);
      float c8[8];
      c8[0] = __builtin_fmaf(w1, blo(a0[0]), w2 * blo(b0[0])); c8[1] = __builtin_fmaf(w1, bhi(a0[0]), w2 * bhi(b0[0]));
      c8[2] = __builtin_fmaf(w1, blo(a0[2]), w2 * blo(b0[2])); c8[3] = __builtin_fmaf(w1, bhi(a0[2]), w2 * bhi(b0[2]));
      c8[4] = __builtin_fmaf(w1, blo(a1[0]), w2 * blo(b1[0])); c8[5] = __builtin_fmaf(w1, bhi(a1[0]), w2 * bhi(b1[0]));
      c8[6] = __builtin_fmaf(w1, blo(a1[2]), w2 * blo(b1[2])); c8[7] = __builtin_fmaf(w1, bhi(a1[2]), w2 * bhi(b1[2]));
#pragma unroll
      for (int e = 0; e < 8; ++e) { c8[e] += dppf<XOR1>(c8[e]); c8[e] += dppf<XOR2>(c8[e]); c8[e] += dppf<HMIR>(c8[e]); }
      const v4u af = {pk(c8[0], c8[1]), pk(c8[2], c8[3]), pk(c8[4], c8[5]), pk(c8[6], c8[7])};
      f32x4 d[8];
      float o[8];
#pragma unroll
      for (int j = 0; j < 8; ++j) d[j] = mfma16(af, TO[j], f32x4{0.f, 0.f, 0.f, 0.f});
      KEEP8(d);
#pragma unroll
      for (int j = 0; j < 8; ++j) o[j] = d[j][0];
      const bool hi = g & 1;
      lds_st<f32x4>(L, LA_T2 + w * 1024 + lane * 16, f32x4{hi ? o[4] : o[0], hi ? o[5] : o[1], hi ? o[6] : o[2], hi ? o[7] : o[3]});
      BAR();
      if ((w & 3) == 0) {
        const f32x4 r4 = (lds_ld<f32x4>(L, LA_T2 + w * 1024 + lane * 16) + lds_ld<f32x4>(L, LA_T2 + (w + 1) * 1024 + lane * 16)) +
                         (lds_ld<f32x4>(L, LA_T2 + (w + 2) * 1024 + lane * 16) + lds_ld<f32x4>(L, LA_T2 + (w + 3) * 1024 + lane * 16));
        st128(piece(r4[0], r4[1], r4[2], r4[3], tag), ws, ((g >> 1) * 32 + (n >> 2) * 2 + (g & 1)) * 4096 + (n & 3) * 16, WS_FO + (64 + cbB * 8) * 4096 + hB * 128);
      }
      STAMP(3);
    }
  }
  STAMP_FLUSH();
}

}

extern "C" void kernel_launch(void* const* d_in, const int* in_sizes, int n_in, void* d_out, int out_size, void* d_ws, size_t ws_size,
                              hipStream_t stream) {
  Params p;
  p.xA = d_in[0]; p.ckv = d_in[1]; p.wqA = d_in[2]; p.wuk = d_in[3]; p.wuv = d_in[4]; p.woA = d_in[5];
  p.xB = d_in[6]; p.kc = d_in[7]; p.vc = d_in[8]; p.wqB = d_in[9]; p.woB = d_in[10];
  p.out = d_out; p.ws = d_ws;
  fused_decode<<<256, 512, 0, stream>>>(p);
}
```

```cpp
#define STAMPS 1
#include <hip/hip_runtime.h>
#include <hip/hip_bf16.h>
#include <stdint.h>

#define WIN_B 3
#define WIN_A 8

namespace {

typedef unsigned v4u __attribute__((ext_vector_type(4)));
typedef unsigned v2u __attribute__((ext_vector_type(2)));
typedef float f32x2 __attribute__((ext_vector_type(2)));
typedef float f32x4 __attribute__((ext_vector_type(4)));
typedef float f32x16 __attribute__((ext_vector_type(16)));
typedef __bf16 bf16x2 __attribute__((ext_vector_type(2)));
typedef __bf16 bf16x8 __attribute__((ext_vector_type(8)));
typedef short s16x4 __attribute__((ext_vector_type(4)));
typedef __amdgpu_buffer_rsrc_t rsrc_t;
#define LAS __attribute__((address_space(3)))
#define GAS __attribute__((address_space(1)))

constexpr int AUX_NT = 2, AUX_SC1 = 16;
constexpr float SCL = 0.08838834764831845f * 1.4426950408889634f;
constexpr unsigned SPIN_CAP = 1u << 15;

constexpr int WS_E = 0;
constexpr int WS_G1A = 4096;
constexpr int WS_G1B = WS_G1A + 256 * 1024;
constexpr int WS_QABS = WS_G1B + 256 * 1024;
constexpr int WS_LA = WS_QABS + 128 * 1024;
constexpr int WS_PA = WS_LA + 64 * 1024;
constexpr int WS_CA = WS_PA + 2048 * 1024;
constexpr int WS_OB = WS_CA + 512 * 1024;
constexpr int WS_LB = WS_OB + 384 * 1024;
constexpr int WS_FO = WS_LB + 96 * 1024;
constexpr int WS_END = WS_FO + 512 * 1024;
constexpr int WS_STAMP = 8 << 20;

constexpr int LB_Q = 0;
constexpr int LB_ML = 1024;
constexpr int LB_TAG = 1536;
constexpr int LB_CNT = 1540;
constexpr int LB_O = 2048;
constexpr int LA_MISC = 131072;
constexpr int LA_P = LA_MISC;
constexpr int LA_MX = LA_P + 32 * 144;
constexpr int LA_AL = LA_MX + 512;
constexpr int LA_LS = LA_AL + 128;
constexpr int LA_MF = LA_LS + 512;
constexpr int LA_WT = LA_MF + 128;
constexpr int LA_T1 = 0;
constexpr int LA_T2 = 65536;

struct Params {
  const void* xA; const void* ckv; const void* wqA; const void* wuk; const void* wuv; const void* woA;
  const void* xB; const void* kc; const void* vc; const void* wqB; const void* woB;
  void* out; void* ws;
};

__device__ __forceinline__ rsrc_t mk_rsrc(const void* p, int bytes) { return __builtin_amdgcn_make_buffer_rsrc((void*)p, 0, bytes, 0x00020000); }
template <int AUX> __device__ __forceinline__ v4u ld128(rsrc_t r, int voff, int soff) { return __builtin_amdgcn_raw_buffer_load_b128(r, voff, soff, AUX); }
template <int AUX> __device__ __forceinline__ v2u ld64(rsrc_t r, int voff, int soff) { return __builtin_amdgcn_raw_buffer_load_b64(r, voff, soff, AUX); }
template <int AUX> __device__ __forceinline__ unsigned ld32(rsrc_t r, int voff, int soff) { return __builtin_amdgcn_raw_buffer_load_b32(r, voff, soff, AUX); }
__device__ __forceinline__ void st128(v4u v, rsrc_t r, int voff, int soff) { __builtin_amdgcn_raw_buffer_store_b128(v, r, voff, soff, AUX_SC1); }
__device__ __forceinline__ void st64(v2u v, rsrc_t r, int voff, int soff) { __builtin_amdgcn_raw_buffer_store_b64(v, r, voff, soff, AUX_SC1); }
__device__ __forceinline__ void st32(unsigned v, rsrc_t r, int voff, int soff) { __builtin_amdgcn_raw_buffer_store_b32(v, r, voff, soff, AUX_SC1); }

__device__ __forceinline__ unsigned pk(float lo, float hi) { f32x2 f = {lo, hi}; return __builtin_bit_cast(unsigned, __builtin_convertvector(f, bf16x2)); }
__device__ __forceinline__ float blo(unsigned u) { return __builtin_bit_cast(float, u << 16); }
__device__ __forceinline__ float bhi(unsigned u) { return __builtin_bit_cast(float, u & 0xffff0000u); }
__device__ __forceinline__ float asf(unsigned u) { return __builtin_bit_cast(float, u); }
__device__ __forceinline__ unsigned asu(float f) { return __builtin_bit_cast(unsigned, f); }
__device__ __forceinline__ f32x4 mfma16(v4u a, v4u b, f32x4 c) { return __builtin_amdgcn_mfma_f32_16x16x32_bf16(__builtin_bit_cast(bf16x8, a), __builtin_bit_cast(bf16x8, b), c, 0, 0, 0); }
__device__ __forceinline__ f32x16 mfma32(v4u a, v4u b, f32x16 c) { return __builtin_amdgcn_mfma_f32_32x32x16_bf16(__builtin_bit_cast(bf16x8, a), __builtin_bit_cast(bf16x8, b), c, 0, 0, 0); }
template <int CTRL> __device__ __forceinline__ float dppf(float x) { return asf(__builtin_amdgcn_mov_dpp(asu(x), CTRL, 0xf, 0xf, true)); }
template <int CTRL> __device__ __forceinline__ unsigned dppu(unsigned x) { return __builtin_amdgcn_mov_dpp(x, CTRL, 0xf, 0xf, true); }
constexpr int XOR1 = 0xB1, XOR2 = 0x4E, HMIR = 0x141, ROR8 = 0x128, ROR4 = 0x124;
__device__ __forceinline__ float rows_max(float x) {
  auto s = __builtin_amdgcn_permlane16_swap(asu(x), asu(x), false, false);
  x = fmaxf(asf(s[0]), asf(s[1]));
  auto t = __builtin_amdgcn_permlane32_swap(asu(x), asu(x), false, false);
  return fmaxf(asf(t[0]), asf(t[1]));
}
__device__ __forceinline__ float rows_sum(float x) {
  auto s = __builtin_amdgcn_permlane16_swap(asu(x), asu(x), false, false);
  x = asf(s[0]) + asf(s[1]);
  auto t = __builtin_amdgcn_permlane32_swap(asu(x), asu(x), false, false);
  return asf(t[0]) + asf(t[1]);
}
__device__ __forceinline__ float ex2(float x) { return __builtin_amdgcn_exp2f(x); }
__device__ __forceinline__ float diag_sum(f32x4 d) { return (dppf<0x00>(d[0]) + dppf<0x55>(d[1])) + (dppf<0xAA>(d[2]) + dppf<0xFF>(d[3])); }
template <int J> __device__ __forceinline__ v4u tcol(const v4u* r) {
  constexpr unsigned sel = (J & 1) ? 0x07060302u : 0x05040100u;
  v4u t;
  t[0] = __builtin_amdgcn_perm(r[1][J >> 1], r[0][J >> 1], sel);
  t[1] = __builtin_amdgcn_perm(r[3][J >> 1], r[2][J >> 1], sel);
  t[2] = __builtin_amdgcn_perm(r[5][J >> 1], r[4][J >> 1], sel);
  t[3] = __builtin_amdgcn_perm(r[7][J >> 1], r[6][J >> 1], sel);
  return t;
}
__device__ __forceinline__ void tr8(const v4u* r, v4u* t) {
  t[0] = tcol<0>(r); t[1] = tcol<1>(r); t[2] = tcol<2>(r); t[3] = tcol<3>(r);
  t[4] = tcol<4>(r); t[5] = tcol<5>(r); t[6] = tcol<6>(r); t[7] = tcol<7>(r);
}
__device__ __forceinline__ void gemv8(v4u a, const v4u* r, f32x4* acc) {
  acc[0] = mfma16(a, tcol<0>(r), acc[0]); acc[1] = mfma16(a, tcol<1>(r), acc[1]);
  acc[2] = mfma16(a, tcol<2>(r), acc[2]); acc[3] = mfma16(a, tcol<3>(r), acc[3]);
  acc[4] = mfma16(a, tcol<4>(r), acc[4]); acc[5] = mfma16(a, tcol<5>(r), acc[5]);
  acc[6] = mfma16(a, tcol<6>(r), acc[6]); acc[7] = mfma16(a, tcol<7>(r), acc[7]);
}
__device__ __forceinline__ v4u piece(float a, float b, float c, float d, unsigned tag) { return v4u{pk(a, b), tag, pk(c, d), tag}; }
__device__ __forceinline__ unsigned bad(v4u v, unsigned tag) { return (v[1] ^ tag) | (v[3] ^ tag); }
__device__ __forceinline__ bool all_ok(unsigned badbits) { return __builtin_amdgcn_ballot_w64(badbits != 0u) == 0ull; }

#define SB() __builtin_amdgcn_sched_barrier(0)
#define WAIT_VM(n) asm volatile("s_waitcnt vmcnt(%0)" ::"n"(n) : "memory")
#define BAR() do { SB(); asm volatile("s_waitcnt lgkmcnt(0)\n\ts_barrier" ::: "memory"); SB(); } while (0)
#define KEEP8(a) asm volatile("" : "+v"(a[0]), "+v"(a[1]), "+v"(a[2]), "+v"(a[3]), "+v"(a[4]), "+v"(a[5]), "+v"(a[6]), "+v"(a[7]))
#define ORDER() asm volatile("" ::: "memory")

template <typename T> __device__ __forceinline__ T lds_ld(LAS char* l, int off) { return *(LAS T*)(l + off); }
template <typename T> __device__ __forceinline__ void lds_st(LAS char* l, int off, T v) { *(LAS T*)(l + off) = v; }
__device__ __forceinline__ v2u lds_tr(LAS char* l, int off) { return __builtin_bit_cast(v2u, __builtin_amdgcn_ds_read_tr16_b64_v4i16((LAS s16x4*)(l + off))); }
__device__ __forceinline__ void dma_row(rsrc_t r, int voff, int soff, unsigned ldsaddr) {
  unsigned keep;
  asm volatile("s_mov_b32 %0, m0\n\ts_mov_b32 m0, %4\n\ts_nop 0\n\tbuffer_load_dwordx4 %1, %2, %3 offen nt lds\n\ts_mov_b32 m0, %0"
               : "=&s"(keep) : "v"(voff), "s"(r), "s"(soff), "s"(ldsaddr) : "memory");
}
__device__ __forceinline__ void lds_arrive(LAS char* l, int off, int lane) {
  asm volatile("s_waitcnt lgkmcnt(0)" ::: "memory");
  if (lane == 0) __hip_atomic_fetch_add((LAS unsigned*)(l + off), 1u, __ATOMIC_RELAXED, __HIP_MEMORY_SCOPE_WORKGROUP);
  asm volatile("" ::: "memory");
}
__device__ __forceinline__ void lds_wait(LAS char* l, int off, unsigned want) {
  _Pragma("nounroll") for (unsigned spin = 0; spin < (SPIN_CAP << 6); ++spin) {
    if (__builtin_amdgcn_readfirstlane(*(volatile LAS unsigned*)(l + off)) >= want) break;
  }
  asm volatile("" ::: "memory");
}
__device__ __forceinline__ constexpr int fsw(int row) { return ((row & 3) << 2) | ((row >> 2) & 3); }

#define NSTAMP 16
#define STAMP(i) do { SB(); unsigned long long t_; asm volatile("s_memrealtime %0\n\ts_waitcnt lgkmcnt(0)" : "=s"(t_)::"memory"); stampv = (lane == (i)) ? (unsigned)t_ : stampv; SB(); } while (0)
#define STAMP_FLUSH() do { if (lane < NSTAMP) ((GAS unsigned*)((GAS char*)p.ws + WS_STAMP))[(blk * 8 + w) * NSTAMP + lane] = stampv; } while (0)

__device__ __forceinline__ float qk4(v4u k, v4u q) { return diag_sum(mfma16(k, q, f32x4{0.f, 0.f, 0.f, 0.f})); }

__global__ void __launch_bounds__(512, 2) fused_decode(Params p) {
  __shared__ __attribute__((aligned(16))) char smem[163840];
  LAS char* L = (LAS char*)smem;
  const int lane = threadIdx.x & 63, w = __builtin_amdgcn_readfirstlane(threadIdx.x >> 6);
  const int blk = blockIdx.x, x = blk & 7, y = blk >> 3;
  const int g = lane >> 4, n = lane & 15;
  unsigned stampv = 0;
  STAMP(0);
  asm volatile("" : "+s"(p.xA), "+s"(p.ckv), "+s"(p.wqA), "+s"(p.wuk), "+s"(p.wuv), "+s"(p.woA), "+s"(p.xB), "+s"(p.kc), "+s"(p.vc), "+s"(p.wqB), "+s"(p.woB), "+s"(p.out), "+s"(p.ws));
  const rsrc_t ws = mk_rsrc(p.ws, WS_END);
  unsigned tag = 0;
  const int voffV = lane * 16;
  const int voffW8 = g * 8 * 8192 + n * 16;
  const int voffW1 = g * 8 * 1024 + n * 16;

  if (w == 0) {
    const int hq = 4 * x + (y >> 3), mq = y & 7;
    const rsrc_t rxa = mk_rsrc(p.xA, 2048), rxb = mk_rsrc(p.xB, 2048);
    const rsrc_t rwa = mk_rsrc(p.wqA, 4 << 20), rwb = mk_rsrc(p.wqB, 4 << 20), rwk = mk_rsrc(p.wuk, 4 << 20);
    const int xo = (n >> 3) * 1024 + g * 16;
    v4u xa[2], xb[2], wa[16], wb[16];
    const unsigned ep = ld32<AUX_SC1>(ws, 0, WS_E);
    xa[0] = ld128<0>(rxa, xo, mq * 128); xa[1] = ld128<0>(rxa, xo, mq * 128 + 64);
#pragma unroll
    for (int i = 0; i < 16; ++i) wa[i] = ld128<AUX_NT>(rwa, voffW8, (64 * mq + 32 * (i >> 3) + (i & 7)) * 8192 + hq * 256);
    xb[0] = ld128<0>(rxb, xo, mq * 128); xb[1] = ld128<0>(rxb, xo, mq * 128 + 64);
#pragma unroll
    for (int i = 0; i < 16; ++i) wb[i] = ld128<AUX_NT>(rwb, voffW8, (64 * mq + 32 * (i >> 3) + (i & 7)) * 8192 + hq * 256);
    SB();
    STAMP(1);
    tag = __builtin_amdgcn_readfirstlane(ep) + 1u;
    {
      f32x4 acc[8];
#pragma unroll
      for (int j = 0; j < 8; ++j) acc[j] = f32x4{0.f, 0.f, 0.f, 0.f};
      gemv8(xa[0], wa, acc); gemv8(xa[1], wa + 8, acc); KEEP8(acc);
      const bool hi = g & 1;
      st128(piece(hi ? acc[4][0] : acc[0][0], hi ? acc[5][0] : acc[1][0], hi ? acc[6][0] : acc[2][0], hi ? acc[7][0] : acc[3][0], tag), ws, voffV, WS_G1A + (hq * 8 + mq) * 1024);
    }
    SB();
    STAMP(2);
    v4u wk[16];
    const int voffK8 = (n >> 2) * 16 * 8192 + (4 * g + (n & 3)) * 16;
#pragma unroll
    for (int i = 0; i < 16; ++i) wk[i] = ld128<AUX_NT>(rwk, voffK8, (64 * mq + i) * 8192 + hq * 256);
    BAR();
    {
      f32x4 acc[8];
#pragma unroll
      for (int j = 0; j < 8; ++j) acc[j] = f32x4{0.f, 0.f, 0.f, 0.f};
      gemv8(xb[0], wb, acc); gemv8(xb[1], wb + 8, acc); KEEP8(acc);
      const bool hi = g & 1;
      st128(piece(hi ? acc[4][0] : acc[0][0], hi ? acc[5][0] : acc[1][0], hi ? acc[6][0] : acc[2][0], hi ? acc[7][0] : acc[3][0], tag), ws, voffV, WS_G1B + (hq * 8 + mq) * 1024);
    }
    SB();
    STAMP(3);
    v4u qa[8];
    {
      const int go = ((((n >> 2) & 1) * 2 + (n >> 3)) * 16 + 4 * g + (n & 3)) * 16;
      _Pragma("nounroll") for (unsigned spin = 0; spin < SPIN_CAP; ++spin) {
        unsigned bb = 0;
#pragma unroll
        for (int k = 0; k < 8; ++k) qa[k] = ld128<AUX_SC1>(ws, go, WS_G1A + (hq * 8 + k) * 1024);
#pragma unroll
        for (int k = 0; k < 8; ++k) bb |= bad(qa[k], tag);
        if (all_ok(bb)) break;
        ORDER();
      }
    }
    STAMP(4);
    float s0 = 0.f, s1 = 0.f, s2 = 0.f, s3 = 0.f;
#pragma unroll
    for (int k = 0; k < 8; ++k) { s0 += blo(qa[k][0]); s1 += bhi(qa[k][0]); s2 += blo(qa[k][2]); s3 += bhi(qa[k][2]); }
    const unsigned o0 = pk(s0, s1), o1 = pk(s2, s3), p0 = dppu<ROR8>(o0), p1 = dppu<ROR8>(o1);
    const bool up = n >> 3;
    const v4u qf = {up ? p0 : o0, up ? p1 : o1, up ? o0 : p0, up ? o1 : p1};
    float r[16];
#pragma unroll
    for (int i = 0; i < 16; ++i) r[i] = qk4(wk[i], qf);
    const bool d1 = n & 1, d2 = n & 2;
    const unsigned u0 = d2 ? (d1 ? pk(r[12], r[13]) : pk(r[8], r[9])) : (d1 ? pk(r[4], r[5]) : pk(r[0], r[1]));
    const unsigned u1 = d2 ? (d1 ? pk(r[14], r[15]) : pk(r[10], r[11])) : (d1 ? pk(r[6], r[7]) : pk(r[2], r[3]));
    if (n < 8) st128(v4u{u0, tag, u1, tag}, ws, (n >> 2) * 65536 + g * 64 + (n & 3) * 16, WS_QABS + hq * 2048 + mq * 256);
    STAMP(5);
  } else {
    BAR();
    STAMP(1);
  }

  if (y < 24) {
    const int b = y >= 12 ? 1 : 0, s = y - 12 * b;
    const rsrc_t rk = mk_rsrc((const char*)p.kc + (size_t)(b * 8 + x) * (2 << 20), 2 << 20);
    const rsrc_t rv = mk_rsrc((const char*)p.vc + (size_t)(b * 8 + x) * (2 << 20), 2 << 20);
    const int voffK = (n >> 2) * 256 + (4 * g + (n & 3)) * 16;
    if (w != 0) {
      const int g0 = (s * 7 + w - 1) * 1024;
      v4u K[24], V[24];
#pragma unroll
      for (int i = 0; i < 24; ++i) {
        if (i == 1) WAIT_VM(0);
        if (i > WIN_B) WAIT_VM(WIN_B - 1);
        K[i] = ld128<AUX_NT>(rk, voffK, g0 + i * (84 * 1024));
        SB();
      }
#define LDV(i) do { SB(); WAIT_VM(WIN_B - 1); V[i] = ld128<AUX_NT>(rv, voffV, g0 + (i) * (84 * 1024)); SB(); } while (0)
      LDV(0);
      STAMP(2);
      BAR();
      STAMP(3);
      const v4u qf = lds_ld<v4u>(L, LB_Q + lane * 16);
      tag = __builtin_amdgcn_readfirstlane(lds_ld<unsigned>(L, LB_TAG));
      float sc[24];
#pragma unroll
      for (int j = 0; j < 6; ++j) {
#pragma unroll
        for (int i = 4 * j; i < 4 * j + 4; ++i) sc[i] = qk4(K[i], qf);
        LDV(j + 1);
      }
      float m = sc[0];
#pragma unroll
      for (int i = 1; i < 24; ++i) m = fmaxf(m, sc[i]);
      m = rows_max(m) * SCL;
      LDV(7);
      float l = 0.f;
      v4u P[3];
#pragma unroll
      for (int t = 0; t < 3; ++t) {
#pragma unroll
        for (int i = 8 * t; i < 8 * t + 8; ++i) { sc[i] = ex2(__builtin_fmaf(sc[i], SCL, -m)); l += sc[i]; }
        P[t] = v4u{pk(sc[8 * t], sc[8 * t + 1]), pk(sc[8 * t + 2], sc[8 * t + 3]), pk(sc[8 * t + 4], sc[8 * t + 5]), pk(sc[8 * t + 6], sc[8 * t + 7])};
        LDV(8 + t);
      }
      l = rows_sum(l);
      if (lane < 16 && (n & 3) == 0) lds_st<f32x2>(L, LB_ML + w * 32 + (n >> 2) * 8, f32x2{m, l});
      STAMP(4);
      f32x4 acc[8];
#pragma unroll
      for (int j = 0; j < 8; ++j) acc[j] = f32x4{0.f, 0.f, 0.f, 0.f};
#pragma unroll
      for (int i = 11; i < 13; ++i) LDV(i);
      gemv8(P[0], V, acc);
#pragma unroll
      for (int i = 13; i < 21; ++i) LDV(i);
      gemv8(P[1], V + 8, acc);
#pragma unroll
      for (int i = 21; i < 24; ++i) LDV(i);
#undef LDV
      STAMP(5);
      BAR();
      float M = -INFINITY, Ls = 0.f;
      f32x2 ml[8];
#pragma unroll
      for (int v = 0; v < 8; ++v) { ml[v] = lds_ld<f32x2>(L, LB_ML + v * 32 + g * 8); M = fmaxf(M, ml[v][0]); }
#pragma unroll
      for (int v = 0; v < 8; ++v) Ls = __builtin_fmaf(ex2(ml[v][0] - M), ml[v][1], Ls);
      const float mw = lds_ld<float>(L, LB_ML + w * 32 + g * 8);
      const float f = ex2(mw - M) / Ls;
      if (w == 7 && n == 0) st64(v2u{asu(M + __builtin_amdgcn_logf(Ls)), tag}, ws, g * (24 * 128), WS_LB + ((4 * x * 2 + b) * 12 + s) * 128);
      SB();
      gemv8(P[2], V + 16, acc);
      KEEP8(acc);
      lds_st<f32x4>(L, LB_O + w * 2048 + lane * 32, f32x4{acc[0][0] * f, acc[1][0] * f, acc[2][0] * f, acc[3][0] * f});
      lds_st<f32x4>(L, LB_O + w * 2048 + lane * 32 + 16, f32x4{acc[4][0] * f, acc[5][0] * f, acc[6][0] * f, acc[7][0] * f});
      STAMP(6);
      lds_arrive(L, LB_CNT, lane);
      if (w == 1 || w == 2) {
        lds_wait(L, LB_CNT, 8u);
        STAMP(7);
        const int pi = (w - 1) * 64 + lane;
        f32x4 o = lds_ld<f32x4>(L, LB_O + pi * 16);
#pragma unroll
        for (int v = 1; v < 8; ++v) o += lds_ld<f32x4>(L, LB_O + v * 2048 + pi * 16);
        st128(piece(o[0], o[1], o[2], o[3], tag), ws, (lane >> 5) * (24 * 512) + (lane & 31) * 16, WS_OB + (((4 * x + 2 * (w - 1)) * 2 + b) * 12 + s) * 512);
        STAMP(8);
      } else if (s >= 8 && (w == 3 || w == 4)) {
        const int br = w == 3 ? 1 : 0, u = x * 8 + b * 4 + (s - 8);
        v4u f0, f1;
        _Pragma("nounroll") for (unsigned spin = 0; spin < SPIN_CAP; ++spin) {
          f0 = ld128<AUX_SC1>(ws, (lane >> 2) * 128 + (lane & 3) * 16, WS_FO + (br * 64 + u) * 4096);
          f1 = ld128<AUX_SC1>(ws, (lane >> 2) * 128 + (lane & 3) * 16, WS_FO + (br * 64 + u) * 4096 + 2048);
          if (all_ok(bad(f0, tag) | bad(f1, tag))) break;
          ORDER();
        }
        STAMP(8);
        float o[4] = {blo(f0[0]) + blo(f1[0]), bhi(f0[0]) + bhi(f1[0]), blo(f0[2]) + blo(f1[2]), bhi(f0[2]) + bhi(f1[2])};
#pragma unroll
        for (int e = 0; e < 4; ++e) { o[e] += dppf<ROR4>(o[e]); o[e] += dppf<ROR8>(o[e]); o[e] = rows_sum(o[e]); }
        if (lane < 4) {
          const int col = (u >> 5) * 1024 + br * 512 + ((u >> 3) & 3) * 128 + (((u >> 1) & 3) * 4 + lane) * 8 + (u & 1) * 4;
          *(GAS v2u*)((GAS char*)p.out + col * 2) = v2u{pk(o[0], o[1]), pk(o[2], o[3])};
        }
        if (u == 0 && br == 0 && lane == 0) st32(tag, ws, 0, WS_E);
        STAMP(9);
      }
    } else {
      v4u qb[16];
      {
        const int go = (n >> 2) * 8192 + (4 * g + (n & 3)) * 16;
        _Pragma("nounroll") for (unsigned spin = 0; spin < SPIN_CAP; ++spin) {
          unsigned bb = 0;
#pragma unroll
          for (int k = 0; k < 16; ++k) qb[k] = ld128<AUX_SC1>(ws, go, WS_G1B + x * 32768 + (k >> 1) * 1024 + (b * 2 + (k & 1)) * 256);
#pragma unroll
          for (int k = 0; k < 16; ++k) bb |= bad(qb[k], tag);
          if (all_ok(bb)) break;
          ORDER();
        }
      }
      float q8[8];
#pragma unroll
      for (int e = 0; e < 8; ++e) q8[e] = 0.f;
#pragma unroll
      for (int k = 0; k < 16; ++k) { const int e = (k & 1) * 4; q8[e] += blo(qb[k][0]); q8[e + 1] += bhi(qb[k][0]); q8[e + 2] += blo(qb[k][2]); q8[e + 3] += bhi(qb[k][2]); }
      const v4u qf = {pk(q8[0], q8[1]), pk(q8[2], q8[3]), pk(q8[4], q8[5]), pk(q8[6], q8[7])};
      lds_st<v4u>(L, LB_Q + lane * 16, qf);
      if (lane == 0) lds_st<v2u>(L, LB_TAG, v2u{tag, s >= 8 ? 0u : 1u});
      STAMP(6);
      if (s >= 8) {
        const int g0 = (2016 + (s - 8) * 8) * 1024;
        v4u K[8], V[8];
#pragma unroll
        for (int i = 0; i < 8; ++i) K[i] = ld128<AUX_NT>(rk, voffK, g0 + i * 1024);
#pragma unroll
        for (int i = 0; i < 8; ++i) V[i] = ld128<AUX_NT>(rv, voffV, g0 + i * 1024);
        SB();
        BAR();
        float sc[8];
#pragma unroll
        for (int i = 0; i < 8; ++i) sc[i] = qk4(K[i], qf);
        float m = sc[0];
#pragma unroll
        for (int i = 1; i < 8; ++i) m = fmaxf(m, sc[i]);
        m = rows_max(m) * SCL;
        float l = 0.f;
#pragma unroll
        for (int i = 0; i < 8; ++i) { sc[i] = ex2(__builtin_fmaf(sc[i], SCL, -m)); l += sc[i]; }
        const v4u P = {pk(sc[0], sc[1]), pk(sc[2], sc[3]), pk(sc[4], sc[5]), pk(sc[6], sc[7])};
        l = rows_sum(l);
        if (lane < 16 && (n & 3) == 0) lds_st<f32x2>(L, LB_ML + (n >> 2) * 8, f32x2{m, l});
        f32x4 acc[8];
#pragma unroll
        for (int j = 0; j < 8; ++j) acc[j] = f32x4{0.f, 0.f, 0.f, 0.f};
        gemv8(P, V, acc);
        KEEP8(acc);
        BAR();
        float M = -INFINITY, Ls = 0.f;
        f32x2 ml[8];
#pragma unroll
        for (int v = 0; v < 8; ++v) { ml[v] = lds_ld<f32x2>(L, LB_ML + v * 32 + g * 8); M = fmaxf(M, ml[v][0]); }
#pragma unroll
        for (int v = 0; v < 8; ++v) Ls = __builtin_fmaf(ex2(ml[v][0] - M), ml[v][1], Ls);
        const float f = ex2(ml[0][0] - M) / Ls;
        lds_st<f32x4>(L, LB_O + lane * 32, f32x4{acc[0][0] * f, acc[1][0] * f, acc[2][0] * f, acc[3][0] * f});
        lds_st<f32x4>(L, LB_O + lane * 32 + 16, f32x4{acc[4][0] * f, acc[5][0] * f, acc[6][0] * f, acc[7][0] * f});
        lds_arrive(L, LB_CNT, lane);
      } else {
        if (lane < 4) lds_st<f32x2>(L, LB_ML + lane * 8, f32x2{-INFINITY, 0.f});
        lds_st<f32x4>(L, LB_O + lane * 32, f32x4{0.f, 0.f, 0.f, 0.f});
        lds_st<f32x4>(L, LB_O + lane * 32 + 16, f32x4{0.f, 0.f, 0.f, 0.f});
        BAR();
        BAR();
        const int idx = b * 8 + s, h = 4 * x + (idx >> 2), cb = idx & 3;
        const rsrc_t rwo = mk_rsrc(p.woA, 4 << 20);
        STAMP(7);
        v4u ca[16], W[32];
        const int go = ((((n >> 2) & 1) * 2 + (n >> 3)) * 16 + 4 * (n & 3) + g) * 16;
#pragma unroll
        for (int k = 0; k < 16; ++k) ca[k] = ld128<AUX_SC1>(ws, go, WS_CA + (h * 16 + k) * 1024);
#pragma unroll
        for (int i = 0; i < 32; ++i) W[i] = ld128<AUX_NT>(rwo, voffW1, (h * 128 + (i >> 3) * 32 + (i & 7)) * 1024 + cb * 256);
        SB();
        {
          unsigned bb = 0;
#pragma unroll
          for (int k = 0; k < 16; ++k) bb |= bad(ca[k], tag);
          if (!all_ok(bb)) {
            _Pragma("nounroll") for (unsigned spin = 0; spin < SPIN_CAP; ++spin) {
              ORDER();
              bb = 0;
#pragma unroll
              for (int k = 0; k < 16; ++k) ca[k] = ld128<AUX_SC1>(ws, go, WS_CA + (h * 16 + k) * 1024);
#pragma unroll
              for (int k = 0; k < 16; ++k) bb |= bad(ca[k], tag);
              if (all_ok(bb)) break;
            }
          }
        }
        STAMP(8);
        float s0 = 0.f, s1 = 0.f, s2 = 0.f, s3 = 0.f;
#pragma unroll
        for (int k = 0; k < 16; ++k) { s0 += blo(ca[k][0]); s1 += bhi(ca[k][0]); s2 += blo(ca[k][2]); s3 += bhi(ca[k][2]); }
        const unsigned o0 = pk(s0, s1), o1 = pk(s2, s3), p0 = dppu<ROR8>(o0), p1 = dppu<ROR8>(o1);
        const bool up = n >> 3;
        const v4u cf = {up ? p0 : o0, up ? p1 : o1, up ? o0 : p0, up ? o1 : p1};
        f32x4 acc[8];
#pragma unroll
        for (int j = 0; j < 8; ++j) acc[j] = f32x4{0.f, 0.f, 0.f, 0.f};
#pragma unroll
        for (int kq = 0; kq < 4; ++kq) {
          const bool on = (n & 3) == kq;
          gemv8(v4u{on ? cf[0] : 0u, on ? cf[1] : 0u, on ? cf[2] : 0u, on ? cf[3] : 0u}, W + 8 * kq, acc);
        }
        float o[8];
#pragma unroll
        for (int j = 0; j < 8; ++j) o[j] = (acc[j][0] + acc[j][1]) + (acc[j][2] + acc[j][3]);
        const bool hi = g >> 1;
        st128(piece(hi ? o[4] : o[0], hi ? o[5] : o[1], hi ? o[6] : o[2], hi ? o[7] : o[3], tag), ws,
              ((g & 1) * 32 + (n >> 2) * 2 + (g >> 1)) * 4096 + (n & 3) * 16, WS_FO + cb * 8 * 4096 + h * 128);
        STAMP(9);
      }
    }
  } else {
    const int a8 = y - 24, b = x >> 2, sp = (x & 3) * 8 + a8;
    const rsrc_t rc = mk_rsrc((const char*)p.ckv + (size_t)b * (8 << 20), 8 << 20);
    const unsigned lbase = (unsigned)(size_t)L;
    const int hf = lane >> 5, n32 = lane & 31;
    unsigned ep = 0;
    if (w != 0) ep = ld32<AUX_SC1>(ws, 0, WS_E);
    if (w >= 4) {
      const int v = w - 4;
#pragma unroll
      for (int i = 0; i < 32; ++i) {
        if (i >= WIN_A) WAIT_VM(WIN_A - 1);
        dma_row(rc, voffV ^ (((v << 2) | (i & 3)) << 4), ((i >> 4) * 2048 + (i & 15) * 128 + sp * 4 + v) * 1024, lbase + (i >> 4) * 65536 + (4 * (i & 15) + v) * 1024);
      }
      STAMP(2);
    }
    BAR();
    if (w != 0) tag = __builtin_amdgcn_readfirstlane(ep) + 1u;
    {
      v4u qr[8];
      _Pragma("nounroll") for (unsigned spin = 0; spin < SPIN_CAP; ++spin) {
        unsigned bb = 0;
#pragma unroll
        for (int i = 0; i < 8; ++i) qr[i] = ld128<AUX_SC1>(ws, voffV, WS_QABS + b * 65536 + (8 * w + i) * 1024);
#pragma unroll
        for (int i = 0; i < 8; ++i) bb |= bad(qr[i], tag);
        if (all_ok(bb)) break;
        ORDER();
      }
      STAMP(6);
#pragma unroll
      for (int i = 0; i < 8; ++i)
        lds_st<v2u>(L, LA_MISC + (4 * w + (i >> 1)) * 1024 + (i & 1) * 512 + ((lane * 8) ^ ((((i >> 1) << 2) | (w & 3)) << 4)), v2u{qr[i][0], qr[i][2]});
    }
    BAR();
    STAMP(7);
    v4u WU[8];
    const rsrc_t rwu = mk_rsrc(p.wuv, 4 << 20);
#define LDU(i) WU[i] = ld128<AUX_NT>(rwu, voffW8, (64 * a8 + 32 * (w >> 2) + (i)) * 8192 + (4 * x + (w & 3)) * 256)
#define DMA(r, i) dma_row(rc, voffV ^ ((((w & 3) << 2) | ((2 * (i) + (w >> 2)) & 3)) << 4), ((r) * 2048 + (2 * (i) + (w >> 2)) * 128 + sp * 4 + (w & 3)) * 1024, lbase + ((r) & 1) * 65536 + (8 * (i) + w) * 1024)
    LDU(0); LDU(1); LDU(2);
    SB();
    const int t = w & 3, hh = w >> 2;
    const int fn = (((n & 3) << 2) | ((n >> 2) & 3)) << 4;
    v4u qf[16];
#pragma unroll
    for (int ks = 0; ks < 16; ++ks) qf[ks] = lds_ld<v4u>(L, LA_MISC + (((16 * hh + n) * 1024 + g * 256 + fn) ^ (ks << 4)));
    const int qkbase = (16 * t + n) * 1024 + g * 256 + fn;
    int trb[4];
    {
      const int blk2 = (lane >> 4) & 1, q = (lane & 15) >> 2, pp = lane & 3;
#pragma unroll
      for (int e = 0; e < 4; ++e) {
        const int ct = e >> 1, tt = e & 1;
        const int chunk = 8 * w + 4 * ct + 2 * blk2 + (pp >> 1);
        trb[e] = (8 * hf + 4 * tt + q) * 1024 + (chunk >> 4) * 256 + (((chunk & 15) ^ ((q << 2) | (2 * hf + tt))) << 4) + 8 * (pp & 1);
      }
    }
    f32x16 acc0, acc1;
#pragma unroll
    for (int e = 0; e < 16; ++e) { acc0[e] = 0.f; acc1[e] = 0.f; }
    float mrun = -INFINITY, lrun = 0.f;
#pragma unroll
    for (int r = 0; r < 4; ++r) {
      const int bo = (r & 1) * 65536;
      if (r == 2) WAIT_VM(8); else if (r == 3) WAIT_VM(0);
      BAR();
      if (r == 0) STAMP(8);
      if (r == 2) STAMP(9);
      if (r == 3) STAMP(10);
      if (r == 1) { DMA(2, 0); DMA(2, 1); DMA(2, 2); SB(); }
      f32x4 sa = {0.f, 0.f, 0.f, 0.f};
      {
        v4u cf[8];
#pragma unroll
        for (int ks = 0; ks < 8; ++ks) cf[ks] = lds_ld<v4u>(L, bo + (qkbase ^ (ks << 4)));
        SB();
#pragma unroll
        for (int ks = 0; ks < 16; ++ks) {
          sa = mfma16(cf[ks & 7], qf[ks], sa);
          if (ks < 8) cf[ks] = lds_ld<v4u>(L, bo + (qkbase ^ ((ks + 8) << 4)));
          SB();
        }
      }
      if (r == 0) { LDU(3); LDU(4); LDU(5); SB(); }
      if (r == 1) { DMA(2, 3); DMA(2, 4); DMA(2, 5); SB(); }
      const float mx = rows_max(fmaxf(fmaxf(sa[0], sa[1]), fmaxf(sa[2], sa[3])));
      if (g == 0) lds_st<float>(L, LA_MX + (16 * hh + n) * 16 + t * 4, mx);
      BAR();
      const f32x4 m4 = lds_ld<f32x4>(L, LA_MX + (16 * hh + n) * 16);
      const float mnew = fmaxf(mrun, fmaxf(fmaxf(m4[0], m4[1]), fmaxf(m4[2], m4[3])) * SCL);
      const float al = ex2(mrun - mnew);
      mrun = mnew;
      const float p0 = ex2(__builtin_fmaf(sa[0], SCL, -mnew)), p1 = ex2(__builtin_fmaf(sa[1], SCL, -mnew));
      const float p2 = ex2(__builtin_fmaf(sa[2], SCL, -mnew)), p3 = ex2(__builtin_fmaf(sa[3], SCL, -mnew));
      lrun = __builtin_fmaf(lrun, al, (p0 + p1) + (p2 + p3));
      lds_st<v2u>(L, LA_P + (16 * hh + n) * 144 + (16 * t + 4 * g) * 2, v2u{pk(p0, p1), pk(p2, p3)});
      if (r > 0 && t == 0 && g == 0) lds_st<float>(L, LA_AL + (16 * hh + n) * 4, al);
      if (r == 3) {
        const float ls = rows_sum(lrun);
        if (g == 0) lds_st<float>(L, LA_LS + (16 * hh + n) * 16 + t * 4, ls);
        if (t == 0 && g == 0) lds_st<float>(L, LA_MF + (16 * hh + n) * 4, mrun);
      }
      if (r == 0) { LDU(6); LDU(7); }
      if (r == 1) { DMA(2, 6); DMA(2, 7); }
      BAR();
      if (r > 0) {
#pragma unroll
        for (int q = 0; q < 4; ++q) {
          const f32x4 a4 = lds_ld<f32x4>(L, LA_AL + (8 * q + 4 * hf) * 4);
#pragma unroll
          for (int e = 0; e < 4; ++e) { acc0[4 * q + e] *= a4[e]; acc1[4 * q + e] *= a4[e]; }
        }
      }
#pragma unroll
      for (int ks = 0; ks < 4; ++ks) {
        const v4u pf = lds_ld<v4u>(L, LA_P + n32 * 144 + (16 * ks + 8 * hf) * 2);
        const v2u c00 = lds_tr(L, bo + trb[0] + ks * 16384), c01 = lds_tr(L, bo + trb[1] + ks * 16384);
        const v2u c10 = lds_tr(L, bo + trb[2] + ks * 16384), c11 = lds_tr(L, bo + trb[3] + ks * 16384);
        acc0 = mfma32(pf, v4u{c00[0], c00[1], c01[0], c01[1]}, acc0);
        acc1 = mfma32(pf, v4u{c10[0], c10[1], c11[0], c11[1]}, acc1);
      }
      if (r == 1) {
        BAR();
#pragma unroll
        for (int i = 0; i < 8; ++i) DMA(3, i);
      }
    }
    STAMP(11);
#pragma unroll
    for (int q = 0; q < 4; ++q)
      st128(v4u{pk(acc0[4 * q], acc0[4 * q + 1]), pk(acc0[4 * q + 2], acc0[4 * q + 3]), pk(acc1[4 * q], acc1[4 * q + 1]), pk(acc1[4 * q + 2], acc1[4 * q + 3])},
            ws, hf * (8 * 32768) + n32 * 16, WS_PA + ((2 * q * 8 + w) * 64 + b * 32 + sp) * 512);
    v4u mlp;
    if (w == 0) {
      const f32x2 mm = lds_ld<f32x2>(L, LA_MF + n * 8);
      const f32x4 l0 = lds_ld<f32x4>(L, LA_LS + n * 32), l1 = lds_ld<f32x4>(L, LA_LS + n * 32 + 16);
      const float la = (l0[0] + l0[1]) + (l0[2] + l0[3]), lb = (l1[0] + l1[1]) + (l1[2] + l1[3]);
      mlp = v4u{asu(g ? la : mm[0]), tag, asu(g ? lb : mm[1]), tag};
    }
    v4u TU[8];
    tr8(WU, TU);
    KEEP8(TU);
    WAIT_VM(0);
    BAR();
    if (w == 0 && lane < 32) st128(mlp, ws, (n >> 1) * 8192 + g * 32 + (n & 1) * 16, WS_LA + b * 4096 + sp * 128);
    STAMP(12);
    {
      const int bb_ = w >> 2;
      v4u mv, lv;
      _Pragma("nounroll") for (unsigned spin = 0; spin < SPIN_CAP; ++spin) {
        mv = ld128<AUX_SC1>(ws, (lane >> 1) * 128 + (lane & 1) * 16, WS_LA + x * 8192 + bb_ * 4096);
        lv = ld128<AUX_SC1>(ws, (lane >> 1) * 128 + (lane & 1) * 16, WS_LA + x * 8192 + bb_ * 4096 + 32);
        if (all_ok(bad(mv, tag) | bad(lv, tag))) break;
        ORDER();
      }
      ORDER();
      v4u pa[4];
#pragma unroll
      for (int i = 0; i < 4; ++i) pa[i] = ld128<AUX_SC1>(ws, voffV, WS_PA + ((x * 8 + a8) * 64 + w * 8 + 2 * i) * 512);
      SB();
      STAMP(13);
      float wt[2];
#pragma unroll
      for (int e = 0; e < 2; ++e) {
        const float ms = asf(mv[2 * e]), ls = asf(lv[2 * e]);
        float M = fmaxf(ms, dppf<XOR2>(ms)); M = fmaxf(M, dppf<ROR4>(M)); M = fmaxf(M, dppf<ROR8>(M)); M = rows_max(M);
        const float ee = ex2(ms - M);
        float S = ee * ls; S += dppf<XOR2>(S); S += dppf<ROR4>(S); S += dppf<ROR8>(S); S = rows_sum(S);
        wt[e] = ee / S;
      }
      lds_st<f32x2>(L, LA_WT + w * 512 + lane * 8, f32x2{wt[0], wt[1]});
      float c8[8];
#pragma unroll
      for (int e = 0; e < 8; ++e) c8[e] = 0.f;
#pragma unroll
      for (int i = 0; i < 4; ++i) {
        const f32x4 w4 = lds_ld<f32x4>(L, LA_WT + w * 512 + (8 * (w & 3) + 2 * i + hf) * 16);
        c8[0] = __builtin_fmaf(w4[0], blo(pa[i][0]), c8[0]); c8[1] = __builtin_fmaf(w4[1], bhi(pa[i][0]), c8[1]);
        c8[2] = __builtin_fmaf(w4[2], blo(pa[i][1]), c8[2]); c8[3] = __builtin_fmaf(w4[3], bhi(pa[i][1]), c8[3]);
        c8[4] = __builtin_fmaf(w4[0], blo(pa[i][2]), c8[4]); c8[5] = __builtin_fmaf(w4[1], bhi(pa[i][2]), c8[5]);
        c8[6] = __builtin_fmaf(w4[2], blo(pa[i][3]), c8[6]); c8[7] = __builtin_fmaf(w4[3], bhi(pa[i][3]), c8[7]);
      }
#pragma unroll
      for (int e = 0; e < 8; ++e) lds_st<float>(L, LA_T1 + (2 * w + hf) * 1040 + e * 128 + n32 * 4, c8[e]);
    }
    const int hB = 4 * x + (a8 >> 1), cbB = 2 * (a8 & 1) + (w >> 2);
    BAR();
    {
      const f32x4 u0 = lds_ld<f32x4>(L, LA_T1 + n * 1040 + w * 128 + g * 32), u1 = lds_ld<f32x4>(L, LA_T1 + n * 1040 + w * 128 + g * 32 + 16);
      float c8[8] = {u0[0], u0[1], u0[2], u0[3], u1[0], u1[1], u1[2], u1[3]};
#pragma unroll
      for (int e = 0; e < 8; ++e) { c8[e] += dppf<XOR1>(c8[e]); c8[e] += dppf<XOR2>(c8[e]); c8[e] += dppf<HMIR>(c8[e]); }
      const v4u af = {pk(c8[0], c8[1]), pk(c8[2], c8[3]), pk(c8[4], c8[5]), pk(c8[6], c8[7])};
      f32x4 d[8];
      float o[8];
#pragma unroll
      for (int j = 0; j < 8; ++j) d[j] = mfma16(af, TU[j], f32x4{0.f, 0.f, 0.f, 0.f});
      KEEP8(d);
#pragma unroll
      for (int j = 0; j < 8; ++j) o[j] = d[j][0];
      const bool hi = g & 1;
      st128(piece(hi ? o[4] : o[0], hi ? o[5] : o[1], hi ? o[6] : o[2], hi ? o[7] : o[3], tag), ws, voffV, WS_CA + ((4 * x + (w & 3)) * 16 + a8 * 2 + (w >> 2)) * 1024);
    }
    STAMP(14);
    v4u WO[8];
    {
      const rsrc_t rwo = mk_rsrc(p.woB, 4 << 20);
#pragma unroll
      for (int i = 0; i < 8; ++i) WO[i] = ld128<AUX_NT>(rwo, voffW1, (hB * 128 + 32 * (w & 3) + i) * 1024 + cbB * 256);
      SB();
    }
    {
      v4u TO[8];
      tr8(WO, TO);
      KEEP8(TO);
      const int s1 = n & 7, s2 = s1 < 4 ? s1 + 8 : s1;
      const int o1 = ((n >> 3) * 12 + s1) * 512 + g * 32, o2 = ((n >> 3) * 12 + s2) * 512 + g * 32;
      const int ob = WS_OB + hB * (24 * 512) + (w & 3) * 128;
      v4u a0, a1, b0, b1; v2u e1, e2;
      _Pragma("nounroll") for (unsigned spin = 0; spin < SPIN_CAP; ++spin) {
        e1 = ld64<AUX_SC1>(ws, ((n >> 3) * 12 + s1) * 128, WS_LB + hB * (24 * 128));
        e2 = ld64<AUX_SC1>(ws, ((n >> 3) * 12 + s2) * 128, WS_LB + hB * (24 * 128));
        if (all_ok((e1[1] ^ tag) | (e2[1] ^ tag))) break;
        __builtin_amdgcn_s_sleep(2);
        ORDER();
      }
      const float l1 = asf(e1[0]), l2 = asf(e2[0]);
      float M = fmaxf(l1, l2); M = fmaxf(M, dppf<XOR1>(M)); M = fmaxf(M, dppf<XOR2>(M)); M = fmaxf(M, dppf<HMIR>(M));
      float w1 = ex2(l1 - M), w2 = s1 < 4 ? ex2(l2 - M) : 0.f;
      float S = w1 + w2; S += dppf<XOR1>(S); S += dppf<XOR2>(S); S += dppf<HMIR>(S);
      const float inv = __builtin_amdgcn_rcpf(S);
      w1 *= inv; w2 *= inv;
      asm volatile("" : "+v"(w1), "+v"(w2));
      _Pragma("nounroll") for (unsigned spin = 0; spin < SPIN_CAP; ++spin) {
        a0 = ld128<AUX_SC1>(ws, o1, ob); a1 = ld128<AUX_SC1>(ws, o1, ob + 16);
        b0 = ld128<AUX_SC1>(ws, o2, ob); b1 = ld128<AUX_SC1>(ws, o2, ob + 16);
        if (all_ok(bad(a0, tag) | bad(a1, tag) | bad(b0, tag) | bad(b1, tag))) break;
        ORDER();
      }
      STAMP(15);
      float c8[8];
      c8[0] = __builtin_fmaf(w1, blo(a0[0]), w2 * blo(b0[0])); c8[1] = __builtin_fmaf(w1, bhi(a0[0]), w2 * bhi(b0[0]));
      c8[2] = __builtin_fmaf(w1, blo(a0[2]), w2 * blo(b0[2])); c8[3] = __builtin_fmaf(w1, bhi(a0[2]), w2 * bhi(b0[2]));
      c8[4] = __builtin_fmaf(w1, blo(a1[0]), w2 * blo(b1[0])); c8[5] = __builtin_fmaf(w1, bhi(a1[0]), w2 * bhi(b1[0]));
      c8[6] = __builtin_fmaf(w1, blo(a1[2]), w2 * blo(b1[2])); c8[7] = __builtin_fmaf(w1, bhi(a1[2]), w2 * bhi(b1[2]));
#pragma unroll
      for (int e = 0; e < 8; ++e) { c8[e] += dppf<XOR1>(c8[e]); c8[e] += dppf<XOR2>(c8[e]); c8[e] += dppf<HMIR>(c8[e]); }
      const v4u af = {pk(c8[0], c8[1]), pk(c8[2], c8[3]), pk(c8[4], c8[5]), pk(c8[6], c8[7])};
      f32x4 d[8];
      float o[8];
#pragma unroll
      for (int j = 0; j < 8; ++j) d[j] = mfma16(af, TO[j], f32x4{0.f, 0.f, 0.f, 0.f});
      KEEP8(d);
#pragma unroll
      for (int j = 0; j < 8; ++j) o[j] = d[j][0];
      const bool hi = g & 1;
      lds_st<f32x4>(L, LA_T2 + w * 1024 + lane * 16, f32x4{hi ? o[4] : o[0], hi ? o[5] : o[1], hi ? o[6] : o[2], hi ? o[7] : o[3]});
      BAR();
      if ((w & 3) == 0) {
        const f32x4 r4 = (lds_ld<f32x4>(L, LA_T2 + w * 1024 + lane * 16) + lds_ld<f32x4>(L, LA_T2 + (w + 1) * 1024 + lane * 16)) +
                         (lds_ld<f32x4>(L, LA_T2 + (w + 2) * 1024 + lane * 16) + lds_ld<f32x4>(L, LA_T2 + (w + 3) * 1024 + lane * 16));
        st128(piece(r4[0], r4[1], r4[2], r4[3], tag), ws, ((g >> 1) * 32 + (n >> 2) * 2 + (g & 1)) * 4096 + (n & 3) * 16, WS_FO + (64 + cbB * 8) * 4096 + hB * 128);
      }
      STAMP(3);
    }
  }
  STAMP_FLUSH();
}

__global__ void __launch_bounds__(1024) k_delay(const unsigned* stamps) {
  __shared__ unsigned smn[1024], smx[1024];
  const int t = threadIdx.x;
  unsigned mn = 0xFFFFFFFFu, mx = 0;
  for (int i = t; i < 2048; i += 1024) {
    const int blk = i >> 3, w = i & 7, y = blk >> 3;
    const int cc = y < 24 ? (w == 0 ? ((y % 12) < 8 ? 0 : 1) : 2) : (w == 0 ? 3 : (w < 4 ? 4 : 5));
    unsigned a = stamps[i * 16]; mn = a < mn ? a : mn;
    unsigned b = stamps[i * 16 + 9];
    if (cc == 2 && b != 0) mx = b > mx ? b : mx;
  }
  smn[t] = mn; smx[t] = mx; __syncthreads();
  for (int o = 512; o > 0; o >>= 1) { if (t < o) { smn[t] = min(smn[t], smn[t + o]); smx[t] = max(smx[t], smx[t + o]); } __syncthreads(); }
  unsigned span = smx[0] - smn[0]; if (0) span = smx[0]; if (span > 20000) span = 20000;
  if (t == 0) { unsigned long long t0 = __builtin_amdgcn_s_memrealtime();
  while (__builtin_amdgcn_s_memrealtime() - t0 < (unsigned long long)span * 0) __builtin_amdgcn_s_sleep(8); }
}
}

extern "C" void kernel_launch(void* const* d_in, const int* in_sizes, int n_in, void* d_out, int out_size, void* d_ws, size_t ws_size,
                              hipStream_t stream) {
  Params p;
  p.xA = d_in[0]; p.ckv = d_in[1]; p.wqA = d_in[2]; p.wuk = d_in[3]; p.wuv = d_in[4]; p.woA = d_in[5];
  p.xB = d_in[6]; p.kc = d_in[7]; p.vc = d_in[8]; p.wqB = d_in[9]; p.woB = d_in[10];
  p.out = d_out; p.ws = d_ws;
  fused_decode<<<256, 512, 0, stream>>>(p);
  k_delay<<<1, 1024, 0, stream>>>((const unsigned*)((char*)d_ws + WS_STAMP));
}
```

```cpp
#include <hip/hip_runtime.h>
#include <hip/hip_bf16.h>
#include <stdint.h>

#define STAMPS 0
#define WIN_K 6
#define WIN_V 6
#define WIN_A 4
#define NTRIG 4
#define PACE 1
#define PT0 270
#define PD0 31
#define PJ1 12
#define PD1 22
#define PSTEP 35
#define NPRE 10
#define NK2 12
#define WIN_K2 4
#define SLP0 10
#define NEND 0
#define WIN_E 12
#define Q2LAST 9
#define CODE_LINES 276
#define SLP_WO 12
#define SLP_LA 0
#define SLP_QA 12
#define SLP_QB 64
#define Q2SLOT 3
#define WIN_H 16

namespace {

typedef unsigned v4u __attribute__((ext_vector_type(4)));
typedef unsigned v2u __attribute__((ext_vector_type(2)));
typedef float f32x2 __attribute__((ext_vector_type(2)));
typedef float f32x4 __attribute__((ext_vector_type(4)));
typedef float f32x16 __attribute__((ext_vector_type(16)));
typedef __bf16 bf16x2 __attribute__((ext_vector_type(2)));
typedef __bf16 bf16x8 __attribute__((ext_vector_type(8)));
typedef short s16x4 __attribute__((ext_vector_type(4)));
typedef __amdgpu_buffer_rsrc_t rsrc_t;
#define LAS __attribute__((address_space(3)))
#define GAS __attribute__((address_space(1)))

constexpr int AUX_NT = 2, AUX_SC1 = 16;
constexpr float SCL = 0.08838834764831845f * 1.4426950408889634f;
constexpr unsigned SPIN_CAP = 1u << 15;

constexpr int WS_E = 0;
constexpr int WS_G1A = 4096;
constexpr int WS_G1B = WS_G1A + 128 * 1024;
constexpr int WS_QABS = WS_G1B + 128 * 1024;
constexpr int WS_LA = WS_QABS + 128 * 1024;
constexpr int WS_PA = WS_LA + 64 * 1024;
constexpr int WS_CA = WS_PA + 2048 * 1024;
constexpr int WS_OB = WS_CA + 256 * 1024;
constexpr int WS_LB = WS_OB + 384 * 1024;
constexpr int WS_FO = WS_LB + 96 * 1024;
constexpr int WS_END = WS_FO + 512 * 1024;
constexpr int WS_STAMP = 8 << 20;

constexpr int LB_Q = 0;
constexpr int LB_ML = 1024;
constexpr int LB_CNT = 1536;
constexpr int LB_O = 2048;
constexpr int LB_R = 18432;
constexpr int LB_QA = LB_R;
constexpr int LA_MISC = 131072;
constexpr int LA_P = LA_MISC;
constexpr int LA_MX = LA_P + 32 * 272;
constexpr int LA_AL = LA_MX + 512;
constexpr int LA_LS = LA_AL + 128;
constexpr int LA_MF = LA_LS + 512;
constexpr int LA_WT = LA_MF + 128;
constexpr int LA_T1 = 0;
constexpr int LA_T2 = 65536;

struct Params {
  const void* xA; const void* ckv; const void* wqA; const void* wuk; const void* wuv; const void* woA;
  const void* xB; const void* kc; const void* vc; const void* wqB; const void* woB;
  void* out; void* ws;
};

__device__ __forceinline__ rsrc_t mk_rsrc(const void* p, int bytes) { return __builtin_amdgcn_make_buffer_rsrc((void*)p, 0, bytes, 0x00020000); }
template <int AUX> __device__ __forceinline__ v4u ld128(rsrc_t r, int voff, int soff) { return __builtin_amdgcn_raw_buffer_load_b128(r, voff, soff, AUX); }
template <int AUX> __device__ __forceinline__ v2u ld64(rsrc_t r, int voff, int soff) { return __builtin_amdgcn_raw_buffer_load_b64(r, voff, soff, AUX); }
template <int AUX> __device__ __forceinline__ unsigned ld32(rsrc_t r, int voff, int soff) { return __builtin_amdgcn_raw_buffer_load_b32(r, voff, soff, AUX); }
__device__ __forceinline__ void st128(v4u v, rsrc_t r, int voff, int soff) {
  __builtin_amdgcn_raw_buffer_store_b128(v, r, voff, soff, AUX_SC1);
  asm volatile("s_nop 1" ::"v"(v) : "memory");
}
__device__ __forceinline__ void st64(v2u v, rsrc_t r, int voff, int soff) { __builtin_amdgcn_raw_buffer_store_b64(v, r, voff, soff, AUX_SC1); }
__device__ __forceinline__ void st32(unsigned v, rsrc_t r, int voff, int soff) { __builtin_amdgcn_raw_buffer_store_b32(v, r, voff, soff, AUX_SC1); }

__device__ __forceinline__ unsigned pk(float lo, float hi) { f32x2 f = {lo, hi}; return __builtin_bit_cast(unsigned, __builtin_convertvector(f, bf16x2)); }
__device__ __forceinline__ float blo(unsigned u) { return __builtin_bit_cast(float, u << 16); }
__device__ __forceinline__ float bhi(unsigned u) { return __builtin_bit_cast(float, u & 0xffff0000u); }
__device__ __forceinline__ float asf(unsigned u) { return __builtin_bit_cast(float, u); }
__device__ __forceinline__ unsigned asu(float f) { return __builtin_bit_cast(unsigned, f); }
__device__ __forceinline__ f32x4 mfma16(v4u a, v4u b, f32x4 c) { return __builtin_amdgcn_mfma_f32_16x16x32_bf16(__builtin_bit_cast(bf16x8, a), __builtin_bit_cast(bf16x8, b), c, 0, 0, 0); }
__device__ __forceinline__ f32x16 mfma32(v4u a, v4u b, f32x16 c) { return __builtin_amdgcn_mfma_f32_32x32x16_bf16(__builtin_bit_cast(bf16x8, a), __builtin_bit_cast(bf16x8, b), c, 0, 0, 0); }
template <int CTRL> __device__ __forceinline__ float dppf(float x) { return asf(__builtin_amdgcn_mov_dpp(asu(x), CTRL, 0xf, 0xf, true)); }
template <int CTRL> __device__ __forceinline__ unsigned dppu(unsigned x) { return __builtin_amdgcn_mov_dpp(x, CTRL, 0xf, 0xf, true); }
constexpr int XOR1 = 0xB1, XOR2 = 0x4E, HMIR = 0x141, ROR8 = 0x128, ROR4 = 0x124;
__device__ __forceinline__ float rows_max(float x) {
  auto s = __builtin_amdgcn_permlane16_swap(asu(x), asu(x), false, false);
  x = fmaxf(asf(s[0]), asf(s[1]));
  auto t = __builtin_amdgcn_permlane32_swap(asu(x), asu(x), false, false);
  return fmaxf(asf(t[0]), asf(t[1]));
}
__device__ __forceinline__ float rows_sum(float x) {
  auto s = __builtin_amdgcn_permlane16_swap(asu(x), asu(x), false, false);
  x = asf(s[0]) + asf(s[1]);
  auto t = __builtin_amdgcn_permlane32_swap(asu(x), asu(x), false, false);
  return asf(t[0]) + asf(t[1]);
}
__device__ __forceinline__ float ex2(float x) { return __builtin_amdgcn_exp2f(x); }
__device__ __forceinline__ float diag_sum(f32x4 d) { return (dppf<0x00>(d[0]) + dppf<0x55>(d[1])) + (dppf<0xAA>(d[2]) + dppf<0xFF>(d[3])); }
template <int J> __device__ __forceinline__ v4u tcol(const v4u* r) {
  constexpr unsigned sel = (J & 1) ? 0x07060302u : 0x05040100u;
  v4u t;
  t[0] = __builtin_amdgcn_perm(r[1][J >> 1], r[0][J >> 1], sel);
  t[1] = __builtin_amdgcn_perm(r[3][J >> 1], r[2][J >> 1], sel);
  t[2] = __builtin_amdgcn_perm(r[5][J >> 1], r[4][J >> 1], sel);
  t[3] = __builtin_amdgcn_perm(r[7][J >> 1], r[6][J >> 1], sel);
  return t;
}
__device__ __forceinline__ void tr8(const v4u* r, v4u* t) {
  t[0] = tcol<0>(r); t[1] = tcol<1>(r); t[2] = tcol<2>(r); t[3] = tcol<3>(r);
  t[4] = tcol<4>(r); t[5] = tcol<5>(r); t[6] = tcol<6>(r); t[7] = tcol<7>(r);
}
__device__ __forceinline__ void gemv8(v4u a, const v4u* r, f32x4* acc) {
  acc[0] = mfma16(a, tcol<0>(r), acc[0]); acc[1] = mfma16(a, tcol<1>(r), acc[1]);
  acc[2] = mfma16(a, tcol<2>(r), acc[2]); acc[3] = mfma16(a, tcol<3>(r), acc[3]);
  acc[4] = mfma16(a, tcol<4>(r), acc[4]); acc[5] = mfma16(a, tcol<5>(r), acc[5]);
  acc[6] = mfma16(a, tcol<6>(r), acc[6]); acc[7] = mfma16(a, tcol<7>(r), acc[7]);
}
template <int J> __device__ __forceinline__ f32x4 gcol4(v2u a, const v4u* r) {
  constexpr unsigned sel = (J & 1) ? 0x07060302u : 0x05040100u;
  const v2u t = {__builtin_amdgcn_perm(r[1][J >> 1], r[0][J >> 1], sel), __builtin_amdgcn_perm(r[3][J >> 1], r[2][J >> 1], sel)};
  return __builtin_amdgcn_mfma_f32_16x16x16bf16_1k(__builtin_bit_cast(s16x4, a), __builtin_bit_cast(s16x4, t), f32x4{0.f, 0.f, 0.f, 0.f}, 0, 0, 0);
}
__device__ __forceinline__ void gemv4(v2u a, const v4u* r, f32x4* acc) {
  acc[0] = gcol4<0>(a, r); acc[1] = gcol4<1>(a, r); acc[2] = gcol4<2>(a, r); acc[3] = gcol4<3>(a, r);
  acc[4] = gcol4<4>(a, r); acc[5] = gcol4<5>(a, r); acc[6] = gcol4<6>(a, r); acc[7] = gcol4<7>(a, r);
}
__device__ __forceinline__ v4u piece(float a, float b, float c, float d, unsigned tag) { return v4u{pk(a, b), tag, pk(c, d), tag}; }
__device__ __forceinline__ unsigned bad(v4u v, unsigned tag) { return (v[1] ^ tag) | (v[3] ^ tag); }
__device__ __forceinline__ bool all_ok(unsigned badbits) { return __builtin_amdgcn_ballot_w64(badbits != 0u) == 0ull; }

#define SB() __builtin_amdgcn_sched_barrier(0)
#define WAIT_VM(n) asm volatile("s_waitcnt vmcnt(%0)" ::"n"(n) : "memory")
#define BAR() do { SB(); asm volatile("s_waitcnt lgkmcnt(0)\n\ts_barrier" ::: "memory"); SB(); } while (0)
#define KEEP8(a) asm volatile("" : "+v"(a[0]), "+v"(a[1]), "+v"(a[2]), "+v"(a[3]), "+v"(a[4]), "+v"(a[5]), "+v"(a[6]), "+v"(a[7]))
#define ORDER() asm volatile("" ::: "memory")

template <typename T> __device__ __forceinline__ T lds_ld(LAS char* l, int off) { return *(LAS T*)(l + off); }
template <typename T> __device__ __forceinline__ void lds_st(LAS char* l, int off, T v) { *(LAS T*)(l + off) = v; }
__device__ __forceinline__ v2u lds_tr(LAS char* l, int off) { return __builtin_bit_cast(v2u, __builtin_amdgcn_ds_read_tr16_b64_v4i16((LAS s16x4*)(l + off))); }
__device__ __forceinline__ void dma_row(rsrc_t r, int voff, int soff, unsigned ldsaddr) {
  unsigned keep;
  asm volatile("s_mov_b32 %0, m0\n\ts_mov_b32 m0, %4\n\ts_nop 0\n\tbuffer_load_dwordx4 %1, %2, %3 offen nt lds\n\ts_mov_b32 m0, %0"
               : "=&s"(keep) : "v"(voff), "s"(r), "s"(soff), "s"(ldsaddr) : "memory");
}
__device__ __forceinline__ void lds_arrive(LAS char* l, int off, int lane) {
  asm volatile("s_waitcnt lgkmcnt(0)" ::: "memory");
  if (lane == 0) __hip_atomic_fetch_add((LAS unsigned*)(l + off), 1u, __ATOMIC_RELAXED, __HIP_MEMORY_SCOPE_WORKGROUP);
  asm volatile("" ::: "memory");
}
template <int SLP = 0> __device__ __forceinline__ void lds_wait(LAS char* l, int off, unsigned want) {
  _Pragma("nounroll") for (unsigned spin = 0; spin < (SPIN_CAP << 6); ++spin) {
    if (__builtin_amdgcn_readfirstlane(*(volatile LAS unsigned*)(l + off)) >= want) break;
    if (SLP) __builtin_amdgcn_s_sleep(SLP);
  }
  asm volatile("" ::: "memory");
}
__device__ __forceinline__ constexpr int fsw(int row) { return ((row & 3) << 2) | ((row >> 2) & 3); }

#define STAMP(i)
#define TSTAMP(i)
#define STAMP_FLUSH()

template <int V> struct IC { static constexpr int value = V; constexpr operator int() const { return V; } };
template <int A, int B, class F> __device__ __forceinline__ void static_for(F&& f) { if constexpr (A < B) { f(IC<A>{}); static_for<A + 1, B>(f); } }
__device__ __forceinline__ int sel(int mask, int a, int b) { return (a & mask) | (b & ~mask); }
constexpr int halfwin(int w2, int i) { return (w2 + (i & 1)) / 2; }
constexpr int wink(int i) { return halfwin(i < NK2 ? WIN_K2 : WIN_K, i); }
constexpr int winv(int i) { return halfwin(i >= 24 - NEND ? WIN_E : WIN_V, i); }
constexpr int psched(int j) { return PT0 + (j < PJ1 ? j * PD0 : PJ1 * PD0 + (j - PJ1) * PD1); }
template <int J, int W> __device__ __forceinline__ void pace(unsigned t0) {
  asm volatile("s_waitcnt vmcnt(%1)\n\ts_memrealtime vcc\n\ts_waitcnt lgkmcnt(0)\n\ts_sub_u32 vcc_lo, vcc_lo, %0\n\t"
               "s_cmp_gt_u32 vcc_lo, %5\n\ts_cbranch_scc1 1f\n\ts_waitcnt vmcnt(%2)\n\t"
               "s_cmp_gt_u32 vcc_lo, %6\n\ts_cbranch_scc1 1f\n\ts_waitcnt vmcnt(%3)\n\t"
               "s_cmp_gt_u32 vcc_lo, %7\n\ts_cbranch_scc1 1f\n\ts_waitcnt vmcnt(%4)\n1:"
               ::"s"(t0), "n"(W + 1), "n"(W), "n"(W - 1), "n"(W - 2), "n"(psched(J) + PSTEP), "n"(psched(J)), "n"(psched(J) - PSTEP) : "vcc", "scc", "memory");
}
__device__ __forceinline__ float qk4(v4u k, v4u q) { return diag_sum(mfma16(k, q, f32x4{0.f, 0.f, 0.f, 0.f})); }

__global__ void __launch_bounds__(512, 2) __attribute__((aligned(4096))) fused_decode(Params p) {
  __shared__ __attribute__((aligned(16))) char smem[163840];
  LAS char* L = (LAS char*)smem;
  int tid = threadIdx.x;
  const int lane = tid & 63, w = __builtin_amdgcn_readfirstlane(tid >> 6);
  const int blk = blockIdx.x, x = blk & 7, y = blk >> 3;
  const int g = lane >> 4, n = lane & 15;
  STAMP(0);
  unsigned long long tstart64 = 0;
  if (PACE) asm volatile("s_memrealtime %0" : "=s"(tstart64));
  unsigned cpf;
  {
    const unsigned long long pc = __builtin_amdgcn_s_getpc() & ~0xFFFull;
    cpf = ld32<0>(mk_rsrc((const void*)pc, CODE_LINES * 128), (lane & 7) * 128, ((y * 8 + w) % ((CODE_LINES + 7) / 8)) * 1024);
  }
  SB();
  unsigned tag = 0;
  const bool roleB = y < 24;
  const int mB = (y - 24) >> 31;
  const int b = sel(mB, (11 - y) >> 31 & 1, x >> 2), s = y - 12 * b;
  const int m1 = mB & ((s - 8) >> 31);
  const bool q1 = m1 != 0;
  const int hq = 2 * b + sel(m1, 4 * (x ^ 4) + (s >> 2), 4 * x + ((s - 8) >> 1));
  const int mq = s & 3, chalf = s & 1;
  const int a8 = y - 24, sp = (x & 3) * 8 + a8;
  const int ck0 = ((w >> 2) * 128 + sp * 4 + (w & 3)) * 1024;
  int voffQ = sel(mB, sel(m1, g * 4 * 8192 + n * 16, (n >> 2) * 8 * 8192 + (4 * g + (n & 3)) * 16), lane * 16);
  int sb0 = sel(mB, sel(m1, 128 * mq + 16 * w, 256 * chalf + 32 * w) * 8192 + hq * 256, b * (8 << 20) + ck0 + 16 * (256 * 1024));
  int st0 = sel(mB, 8192, 256 * 1024);
  asm volatile("" : "+v"(voffQ), "+s"(sb0), "+s"(st0));
  asm volatile("" : "+s"(p.xA), "+s"(p.ckv), "+s"(p.wqA), "+s"(p.wuk), "+s"(p.wuv), "+s"(p.woA), "+s"(p.xB), "+s"(p.kc), "+s"(p.vc), "+s"(p.wqB), "+s"(p.woB), "+s"(p.out), "+s"(p.ws));
  const rsrc_t rw0 = mk_rsrc(roleB ? (q1 ? p.wqA : p.wuk) : p.ckv, sel(mB, 4 << 20, 16 << 20));
  const rsrc_t ws = mk_rsrc(p.ws, WS_END);
  if (PACE) asm volatile("s_waitcnt lgkmcnt(0)" : "+s"(tstart64));
  const unsigned tstart = (unsigned)tstart64;
  v4u F[4];
  if (!q1) __builtin_amdgcn_s_sleep(SLP0);
#pragma unroll
  for (int i = 0; i < 4; ++i) F[i] = ld128<AUX_NT>(rw0, voffQ, sb0 + i * st0);
  v2u xa = ld64<0>(mk_rsrc(p.xA, 2048), (n >> 3) * 1024 + g * 8, mq * 256 + w * 32);
  v2u xb = ld64<0>(mk_rsrc(p.xB, 2048), (n >> 3) * 1024 + g * 8, mq * 256 + w * 32);
  unsigned ep = ld32<AUX_SC1>(ws, 0, WS_E);
  SB();

  if (!roleB) {
    const int lane = tid & 63, g = lane >> 4, n = lane & 15, voffV = lane * 16;
    const int voffW1 = g * 8 * 1024 + n * 16;
    const rsrc_t rc = mk_rsrc((const char*)p.ckv + (size_t)b * (8 << 20), 8 << 20);
    const unsigned lbase = (unsigned)(size_t)L;
    const int hf = lane >> 5, n32 = lane & 31;
    const int sw0 = voffV ^ ((((w & 3) << 2) | (w >> 2)) << 4), sw1 = sw0 ^ 32;
#define DMA(i) dma_row(rc, ((i) & 1) ? sw1 : sw0, ck0 + (i) * (256 * 1024), lbase + (8 * ((i) & 15) + w) * 1024)
#pragma unroll
    for (int i = 0; i < 16; ++i) {
      if (i < 2) WAIT_VM(WIN_A + 2); else WAIT_VM(WIN_A - 1);
      DMA(i);
      TSTAMP(20 + i);
    }
    STAMP(1);
    v4u S[16];
#pragma unroll
    for (int i = 0; i < 4; ++i) S[i] = F[i];
#define LDS_(i) do { if ((i) < 16) S[(i) & 15] = ld128<AUX_NT>(rc, voffV, ck0 + (16 + (i)) * (256 * 1024)); } while (0)
#pragma unroll
    for (int i = 4; i < NPRE; ++i) { SB(); WAIT_VM(WIN_A - 1); LDS_(i); TSTAMP(32 + i); }
    v4u WX[16];
    const int hB = 4 * (x ^ 4) + (a8 >> 1);
    const int mU = (w - 4) >> 31;
    const rsrc_t rwx = mk_rsrc(w < 4 ? p.wuv : p.woB, 4 << 20);
    const int voffX = sel(mU, g * 8 * 8192, g * 8 * 1024) + n * 16;
    const int sx0 = sel(mU, 64 * a8 * 8192 + (4 * x + w) * 256, (hB * 128 + 32 * (w & 3)) * 1024 + (a8 & 1) * 512);
    const int sx1 = sel(mU, 8192, 1024), sx8 = sel(mU, 32 * 8192, 256);
#define LDX(i) WX[i] = ld128<AUX_NT>(rwx, voffX, sx0 + ((i) & 7) * sx1 + ((i) >> 3) * sx8)
    SB();
    STAMP(2);
    asm volatile("" ::"v"(cpf), "v"(xa), "v"(xb));
    tag = __builtin_amdgcn_readfirstlane(ep) + 1u;
    if (w == 0) {
      _Pragma("nounroll") for (unsigned spin = 0; spin < SPIN_CAP; ++spin) {
        const v2u hv = ld64<AUX_SC1>(ws, ((lane >> 3) == x ? lane ^ 8 : lane) * 1024, WS_QABS + b * 65536);
        if (all_ok(hv[1] ^ tag)) break;
        __builtin_amdgcn_s_sleep(2);
        ORDER();
      }
    }
    BAR();
    STAMP(3);
    {
      v4u qr[8];
      _Pragma("nounroll") for (unsigned spin = 0; spin < SPIN_CAP; ++spin) {
        unsigned bb = 0;
#pragma unroll
        for (int i = 0; i < 8; ++i) qr[i] = ld128<AUX_SC1>(ws, voffV, WS_QABS + b * 65536 + (8 * w + i) * 1024);
#pragma unroll
        for (int i = 0; i < 8; ++i) bb |= bad(qr[i], tag);
        if (all_ok(bb)) break;
        ORDER();
      }
      STAMP(6);
#pragma unroll
      for (int i = 0; i < 8; ++i)
        lds_st<v2u>(L, LA_MISC + (4 * w + (i >> 1)) * 1024 + (i & 1) * 512 + ((lane * 8) ^ ((((i >> 1) << 2) | (w & 3)) << 4)), v2u{qr[i][0], qr[i][2]});
    }
    const int t = w & 3, hh = w >> 2;
    const int fn = ((((n & 3) ^ g) << 2) | ((n >> 2) & 3)) << 4;
    int qkb[4], trb[4];
    {
      const int blk2 = (lane >> 4) & 1, q = (lane & 15) >> 2, pp = lane & 3;
#pragma unroll
      for (int e = 0; e < 4; ++e) {
        qkb[e] = ((16 * t + n) * 1024 + fn) ^ (e << 4);
        const int ct = e >> 1, tt = e & 1;
        const int chunk = 8 * w + 4 * ct + 2 * blk2 + (pp >> 1);
        trb[e] = (8 * hf + 4 * tt + q) * 1024 + (chunk >> 4) * 256 + (((chunk & 15) ^ ((q << 2) | (2 * hf + tt))) << 4) + 8 * (pp & 1);
      }
    }
    BAR();
    STAMP(7);
    v4u qf[16], cf[8];
#define RDC(e, ks) cf[e] = lds_ld<v4u>(L, qkb[(ks) & 3] + ((ks) >> 2) * 256)
#define RDT(e) do { const v2u t0_ = lds_tr(L, trb[(2 * (e)) & 3] + ((e) >> 1) * 16384), t1_ = lds_tr(L, trb[(2 * (e) + 1) & 3] + ((e) >> 1) * 16384); \
                    cf[e] = v4u{t0_[0], t0_[1], t1_[0], t1_[1]}; } while (0)
#define FLIP(a) do { _Pragma("unroll") for (int e_ = 0; e_ < 4; ++e_) { a[e_] ^= 65536; asm volatile("" : "+v"(a[e_])); } } while (0)
#pragma unroll
    for (int ks = 0; ks < 16; ++ks) {
      qf[ks] = lds_ld<v4u>(L, LA_MISC + (((16 * hh + n) * 1024 + fn) ^ ((ks & 3) << 4)) + (ks >> 2) * 256);
      if (ks < 8) RDC(ks, ks);
      if ((ks & 1) == 1) LDS_(NPRE + (ks >> 1));
      SB();
    }
    f32x16 acc0, acc1;
#pragma unroll
    for (int e = 0; e < 16; ++e) { acc0[e] = 0.f; acc1[e] = 0.f; }
    float mrun = -INFINITY, lrun = 0.f;
#pragma unroll
    for (int R = 0; R < 2; ++R) {
      f32x4 sa = {0.f, 0.f, 0.f, 0.f}, sb = {0.f, 0.f, 0.f, 0.f};
#pragma unroll
      for (int ks = 0; ks < 16; ++ks) {
        if (ks == 8) { FLIP(qkb); if (R == 1) BAR(); }
        sa = mfma16(cf[ks & 7], qf[ks], sa);
        if (ks < 8) RDC(ks, ks + 8); else RDC(ks - 8, ks - 8);
        if (R == 1 && (ks & 7) == 3) LDX(5 + (ks >> 3));
        SB();
      }
#pragma unroll
      for (int ks = 0; ks < 16; ++ks) {
        sb = mfma16(cf[ks & 7], qf[ks], sb);
        if (ks < 8) RDC(ks, ks + 8); else RDT(ks - 8);
        if (R == 1 && (ks & 7) == 3) LDX(7 + (ks >> 3));
        SB();
      }
      FLIP(qkb); FLIP(trb);
      const float mx = rows_max(fmaxf(fmaxf(fmaxf(sa[0], sa[1]), fmaxf(sa[2], sa[3])), fmaxf(fmaxf(sb[0], sb[1]), fmaxf(sb[2], sb[3]))));
      if (R == 0) BAR();
      if (g == 0) lds_st<float>(L, LA_MX + (16 * hh + n) * 16 + t * 4, mx);
      BAR();
      if (R == 0) STAMP(8); else STAMP(10);
      const f32x4 m4 = lds_ld<f32x4>(L, LA_MX + (16 * hh + n) * 16);
      SB();
      if (R == 0) { lds_st<v4u>(L, w * 1024 + sw0, S[0]); lds_st<v4u>(L, (8 + w) * 1024 + sw1, S[1]); SB(); }
      if (R == 1) { LDX(9); SB(); }
      const float mnew = fmaxf(mrun, fmaxf(fmaxf(m4[0], m4[1]), fmaxf(m4[2], m4[3])) * SCL);
      const float al = ex2(mrun - mnew);
      mrun = mnew;
      float pe[8];
#pragma unroll
      for (int e = 0; e < 4; ++e) { pe[e] = ex2(__builtin_fmaf(sa[e], SCL, -mnew)); pe[4 + e] = ex2(__builtin_fmaf(sb[e], SCL, -mnew)); }
      lds_st<v2u>(L, LA_P + (16 * hh + n) * 272 + (16 * t + 4 * g) * 2, v2u{pk(pe[0], pe[1]), pk(pe[2], pe[3])});
      lds_st<v2u>(L, LA_P + (16 * hh + n) * 272 + 128 + (16 * t + 4 * g) * 2, v2u{pk(pe[4], pe[5]), pk(pe[6], pe[7])});
      if (R == 1 && t == 0 && g == 0) lds_st<float>(L, LA_AL + (16 * hh + n) * 4, al);
      lrun = __builtin_fmaf(lrun, al, ((pe[0] + pe[1]) + (pe[2] + pe[3])) + ((pe[4] + pe[5]) + (pe[6] + pe[7])));
      asm volatile("" : "+v"(lrun));
      if (R == 1) {
        const float ls = rows_sum(lrun);
        if (g == 0) lds_st<float>(L, LA_LS + (16 * hh + n) * 16 + t * 4, ls);
        if (t == 0 && g == 0) lds_st<float>(L, LA_MF + (16 * hh + n) * 4, mrun);
      }
      BAR();
      if (R == 0) STAMP(4); else STAMP(5);
      v4u pf[4];
#pragma unroll
      for (int ks = 0; ks < 4; ++ks) pf[ks] = lds_ld<v4u>(L, LA_P + n32 * 272 + (16 * ks + 8 * hf) * 2);
      if (R == 1) {
#pragma unroll
        for (int q = 0; q < 4; ++q) {
          const f32x4 a4 = lds_ld<f32x4>(L, LA_AL + (8 * q + 4 * hf) * 4);
#pragma unroll
          for (int e = 0; e < 4; ++e) { acc0[4 * q + e] *= a4[e]; acc1[4 * q + e] *= a4[e]; }
        }
      }
      SB();
#pragma unroll
      for (int ks = 0; ks < 4; ++ks) {
        acc0 = mfma32(pf[ks], cf[2 * ks], acc0); asm volatile("" : "+v"(acc0));
        RDT(2 * ks);
        if ((ks & 1) == 0) LDX(10 * R + (ks >> 1));
        if (R == 0 && ks > 0) lds_st<v4u>(L, (16 * ks + w) * 1024 + sw0, S[2 * ks]);
        SB();
        acc1 = mfma32(pf[ks], cf[2 * ks + 1], acc1); asm volatile("" : "+v"(acc1));
        RDT(2 * ks + 1);
        pf[ks] = lds_ld<v4u>(L, LA_P + n32 * 272 + 128 + (16 * ks + 8 * hf) * 2);
        if (R == 0 && ks > 0) lds_st<v4u>(L, (16 * ks + 8 + w) * 1024 + sw1, S[2 * ks + 1]);
        SB();
      }
      FLIP(trb);
      if (R == 0) {
        BAR();
        STAMP(9);
      }
#pragma unroll
      for (int ks = 0; ks < 4; ++ks) {
        acc0 = mfma32(pf[ks], cf[2 * ks], acc0); asm volatile("" : "+v"(acc0));
        if (R == 0) { RDC(2 * ks, 2 * ks); lds_st<v4u>(L, 65536 + (16 * ks + w) * 1024 + sw0, S[8 + 2 * ks]); if (ks < 3) LDX(2 + ks); }
        SB();
        acc1 = mfma32(pf[ks], cf[2 * ks + 1], acc1); asm volatile("" : "+v"(acc1));
        if (R == 0) { RDC(2 * ks + 1, 2 * ks + 1); lds_st<v4u>(L, 65536 + (16 * ks + 8 + w) * 1024 + sw1, S[8 + 2 * ks + 1]); }
        SB();
      }
    }
    STAMP(11);
#pragma unroll
    for (int q = 0; q < 4; ++q)
      st128(v4u{pk(acc0[4 * q], acc0[4 * q + 1]), pk(acc0[4 * q + 2], acc0[4 * q + 3]), pk(acc1[4 * q], acc1[4 * q + 1]), pk(acc1[4 * q + 2], acc1[4 * q + 3])},
            ws, hf * (8 * 32768) + n32 * 16, WS_PA + ((2 * q * 8 + w) * 64 + b * 32 + sp) * 512);
    BAR();
#pragma unroll
    for (int i = 12; i < 16; ++i) LDX(i);
    SB();
    v4u mlp;
    if (w == 1) {
      const f32x2 mm = lds_ld<f32x2>(L, LA_MF + n * 8);
      const f32x4 l0 = lds_ld<f32x4>(L, LA_LS + n * 32), l1 = lds_ld<f32x4>(L, LA_LS + n * 32 + 16);
      const float la = (l0[0] + l0[1]) + (l0[2] + l0[3]), lb = (l1[0] + l1[1]) + (l1[2] + l1[3]);
      mlp = v4u{asu(g ? la : mm[0]), tag, asu(g ? lb : mm[1]), tag};
    }
    v4u TX[16];
    tr8(WX, TX);
    KEEP8(TX);
    WAIT_VM(4);
    BAR();
    if (w == 1 && lane < 32) st128(mlp, ws, (n >> 1) * 8192 + g * 32 + (n & 1) * 16, WS_LA + b * 4096 + sp * 128);
    STAMP(12);
    tr8(WX + 8, TX + 8);
    { v4u* T2 = TX + 8; KEEP8(T2); }
    {
      v4u mv, lv;
      if ((w & 3) == 2) {
        __builtin_amdgcn_s_sleep(SLP_LA);
        _Pragma("nounroll") for (unsigned spin = 0; spin < SPIN_CAP; ++spin) {
          mv = ld128<AUX_SC1>(ws, (lane >> 1) * 128 + (lane & 1) * 16, WS_LA + x * 8192 + hh * 4096);
          lv = ld128<AUX_SC1>(ws, (lane >> 1) * 128 + (lane & 1) * 16, WS_LA + x * 8192 + hh * 4096 + 32);
          if (all_ok(bad(mv, tag) | bad(lv, tag))) break;
          ORDER();
        }
      }
      BAR();
      STAMP(17);
      v4u pa[4];
#pragma unroll
      for (int i = 0; i < 4; ++i) pa[i] = ld128<AUX_SC1>(ws, voffV, WS_PA + ((x * 8 + a8) * 64 + w * 8 + 2 * i) * 512);
      SB();
      if ((w & 3) == 2) {
        float wt[2];
#pragma unroll
        for (int e = 0; e < 2; ++e) {
          const float ms = asf(mv[2 * e]), ls = asf(lv[2 * e]);
          float M = fmaxf(ms, dppf<XOR2>(ms)); M = fmaxf(M, dppf<ROR4>(M)); M = fmaxf(M, dppf<ROR8>(M)); M = rows_max(M);
          const float ee = ex2(ms - M);
          float S_ = ee * ls; S_ += dppf<XOR2>(S_); S_ += dppf<ROR4>(S_); S_ += dppf<ROR8>(S_); S_ = rows_sum(S_);
          wt[e] = ee / S_;
        }
        lds_st<f32x2>(L, LA_WT + hh * 512 + lane * 8, f32x2{wt[0], wt[1]});
      }
      BAR();
      f32x4 w4[4];
#pragma unroll
      for (int i = 0; i < 4; ++i) w4[i] = lds_ld<f32x4>(L, LA_WT + (8 * w + 2 * i + hf) * 16);
      float c8[8];
#pragma unroll
      for (int e = 0; e < 8; ++e) c8[e] = 0.f;
#pragma unroll
      for (int i = 0; i < 4; ++i) {
        c8[0] = __builtin_fmaf(w4[i][0], blo(pa[i][0]), c8[0]); c8[1] = __builtin_fmaf(w4[i][1], bhi(pa[i][0]), c8[1]);
        c8[2] = __builtin_fmaf(w4[i][2], blo(pa[i][1]), c8[2]); c8[3] = __builtin_fmaf(w4[i][3], bhi(pa[i][1]), c8[3]);
        c8[4] = __builtin_fmaf(w4[i][0], blo(pa[i][2]), c8[4]); c8[5] = __builtin_fmaf(w4[i][1], bhi(pa[i][2]), c8[5]);
        c8[6] = __builtin_fmaf(w4[i][2], blo(pa[i][3]), c8[6]); c8[7] = __builtin_fmaf(w4[i][3], bhi(pa[i][3]), c8[7]);
      }
      STAMP(13);
#pragma unroll
      for (int e = 0; e < 4; ++e) {
        auto sw = __builtin_amdgcn_permlane32_swap(asu(c8[e]), asu(c8[e + 4]), false, false);
        lds_st<float>(L, LA_T1 + w * 1040 + hf * 512 + e * 128 + n32 * 4, asf(sw[0]) + asf(sw[1]));
      }
    }
    BAR();
    if (w < 4) {
      const int ta = LA_T1 + ((n >> 3) * 4 + (n & 3)) * 1040 + ((n >> 2) & 1) * 512 + w * 128 + g * 32;
      const f32x4 u0 = lds_ld<f32x4>(L, ta), u1 = lds_ld<f32x4>(L, ta + 16);
      float c8[8] = {u0[0], u0[1], u0[2], u0[3], u1[0], u1[1], u1[2], u1[3]};
#pragma unroll
      for (int e = 0; e < 8; ++e) { c8[e] += dppf<XOR1>(c8[e]); c8[e] += dppf<XOR2>(c8[e]); }
      const v4u af = {pk(c8[0], c8[1]), pk(c8[2], c8[3]), pk(c8[4], c8[5]), pk(c8[6], c8[7])};
      const bool k1 = n & 4;
      const v4u af0 = {k1 ? 0u : af[0], k1 ? 0u : af[1], k1 ? 0u : af[2], k1 ? 0u : af[3]};
      const v4u af1 = {k1 ? af[0] : 0u, k1 ? af[1] : 0u, k1 ? af[2] : 0u, k1 ? af[3] : 0u};
      f32x4 d[8];
#pragma unroll
      for (int j = 0; j < 8; ++j) d[j] = mfma16(af0, TX[j], f32x4{0.f, 0.f, 0.f, 0.f});
#pragma unroll
      for (int j = 0; j < 8; ++j) d[j] = mfma16(af1, TX[8 + j], d[j]);
      KEEP8(d);
      float o[4];
#pragma unroll
      for (int j = 0; j < 4; ++j) { auto sw = __builtin_amdgcn_permlane16_swap(asu(d[j][0]), asu(d[j + 4][0]), false, false); o[j] = asf(sw[0]) + asf(sw[1]); }
      st128(piece(o[0], o[1], o[2], o[3], tag), ws, voffV, WS_CA + ((4 * x + w) * 8 + a8) * 1024);
      STAMP(14);
    } else {
      const int s1 = n & 7, s2 = s1 < 4 ? s1 + 8 : s1;
      const int o1 = ((n >> 3) * 12 + s1) * 512 + g * 32, o2 = ((n >> 3) * 12 + s2) * 512 + g * 32;
      const int ob = WS_OB + hB * (24 * 512) + (w & 3) * 128;
      v4u a0, a1, b0, b1; v2u e1, e2;
      _Pragma("nounroll") for (unsigned spin = 0; spin < SPIN_CAP; ++spin) {
        e1 = ld64<AUX_SC1>(ws, ((n >> 3) * 12 + s1) * 128, WS_LB + hB * (24 * 128));
        e2 = ld64<AUX_SC1>(ws, ((n >> 3) * 12 + s2) * 128, WS_LB + hB * (24 * 128));
        if (all_ok((e1[1] ^ tag) | (e2[1] ^ tag))) break;
        __builtin_amdgcn_s_sleep(4);
        ORDER();
      }
      STAMP(14);
      const float l1 = asf(e1[0]), l2 = asf(e2[0]);
      float M = fmaxf(l1, l2); M = fmaxf(M, dppf<XOR1>(M)); M = fmaxf(M, dppf<XOR2>(M)); M = fmaxf(M, dppf<HMIR>(M));
      float w1 = ex2(l1 - M), w2 = s1 < 4 ? ex2(l2 - M) : 0.f;
      float S_ = w1 + w2; S_ += dppf<XOR1>(S_); S_ += dppf<XOR2>(S_); S_ += dppf<HMIR>(S_);
      const float inv = __builtin_amdgcn_rcpf(S_);
      w1 *= inv; w2 *= inv;
      asm volatile("" : "+v"(w1), "+v"(w2));
      _Pragma("nounroll") for (unsigned spin = 0; spin < SPIN_CAP; ++spin) {
        a0 = ld128<AUX_SC1>(ws, o1, ob); a1 = ld128<AUX_SC1>(ws, o1, ob + 16);
        b0 = ld128<AUX_SC1>(ws, o2, ob); b1 = ld128<AUX_SC1>(ws, o2, ob + 16);
        if (all_ok(bad(a0, tag) | bad(a1, tag) | bad(b0, tag) | bad(b1, tag))) break;
        ORDER();
      }
      STAMP(15);
      float c8[8];
      c8[0] = __builtin_fmaf(w1, blo(a0[0]), w2 * blo(b0[0])); c8[1] = __builtin_fmaf(w1, bhi(a0[0]), w2 * bhi(b0[0]));
      c8[2] = __builtin_fmaf(w1, blo(a0[2]), w2 * blo(b0[2])); c8[3] = __builtin_fmaf(w1, bhi(a0[2]), w2 * bhi(b0[2]));
      c8[4] = __builtin_fmaf(w1, blo(a1[0]), w2 * blo(b1[0])); c8[5] = __builtin_fmaf(w1, bhi(a1[0]), w2 * bhi(b1[0]));
      c8[6] = __builtin_fmaf(w1, blo(a1[2]), w2 * blo(b1[2])); c8[7] = __builtin_fmaf(w1, bhi(a1[2]), w2 * bhi(b1[2]));
#pragma unroll
      for (int e = 0; e < 8; ++e) { c8[e] += dppf<XOR1>(c8[e]); c8[e] += dppf<XOR2>(c8[e]); c8[e] += dppf<HMIR>(c8[e]); }
      const v4u af = {pk(c8[0], c8[1]), pk(c8[2], c8[3]), pk(c8[4], c8[5]), pk(c8[6], c8[7])};
      const bool hi = g & 1;
#pragma unroll
      for (int cq = 0; cq < 2; ++cq) {
        f32x4 d[8];
#pragma unroll
        for (int j = 0; j < 8; ++j) d[j] = mfma16(af, TX[8 * cq + j], f32x4{0.f, 0.f, 0.f, 0.f});
        KEEP8(d);
        lds_st<f32x4>(L, LA_T2 + (cq * 4 + (w & 3)) * 1024 + lane * 16, f32x4{hi ? d[4][0] : d[0][0], hi ? d[5][0] : d[1][0], hi ? d[6][0] : d[2][0], hi ? d[7][0] : d[3][0]});
      }
      BAR();
      if (w < 6) {
        const int tb = LA_T2 + (w - 4) * 4096 + lane * 16;
        const f32x4 r4 = (lds_ld<f32x4>(L, tb) + lds_ld<f32x4>(L, tb + 1024)) + (lds_ld<f32x4>(L, tb + 2048) + lds_ld<f32x4>(L, tb + 3072));
        st128(piece(r4[0], r4[1], r4[2], r4[3], tag), ws, ((g >> 1) * 32 + (n >> 2) * 2 + (g & 1)) * 4096 + (n & 3) * 16,
              WS_FO + (64 + (2 * (a8 & 1) + w - 4) * 8) * 4096 + hB * 128);
      }
      STAMP(16);
    }
    STAMP_FLUSH();
    asm volatile("s_endpgm" : "=v"(F[0]), "=v"(F[1]), "=v"(F[2]), "=v"(F[3]), "=v"(voffQ), "=v"(cpf), "=v"(tid), "=v"(xa), "=v"(xb), "=v"(ep)::"memory");
  }
  {
    const int lane = tid & 63, g = lane >> 4, n = lane & 15, voffV = lane * 16;
    const int voffW1 = g * 8 * 1024 + n * 16;
    const rsrc_t rw1 = mk_rsrc(q1 ? p.wqB : p.wuk, 4 << 20);
    const int sb1 = q1 ? sb0 : sb0 + 4 * 8192;
    v4u Wq[8];
#pragma unroll
    for (int i = 0; i < 4; ++i) Wq[i] = F[i];
    const rsrc_t rk = mk_rsrc((const char*)p.kc + (size_t)(b * 8 + x) * (2 << 20), 2 << 20);
    const rsrc_t rv = mk_rsrc((const char*)p.vc + (size_t)(b * 8 + x) * (2 << 20), 2 << 20);
    const int voffK = (n >> 2) * 256 + (4 * g + (n & 3)) * 16;
#define LDQ(i, wait) do { SB(); WAIT_VM(wait); Wq[i] = ld128<AUX_NT>(rw1, voffQ, sb1 + ((i) - 4) * 8192); SB(); } while (0)
#define Q1PART(br, xv) do { f32x4 acc_[8]; gemv4(xv, Wq + 4 * (br), acc_); KEEP8(acc_); const bool hi_ = g & 1; \
      lds_st<f32x4>(L, LB_R + (br) * 8192 + w * 1024 + lane * 16, f32x4{hi_ ? acc_[4][0] : acc_[0][0], hi_ ? acc_[5][0] : acc_[1][0], hi_ ? acc_[6][0] : acc_[2][0], hi_ ? acc_[7][0] : acc_[3][0]}); \
      lds_arrive(L, LB_CNT + 4 + 4 * (br), lane); } while (0)
#define Q2PART(qf_) do { float r_[8]; _Pragma("unroll") for (int i_ = 0; i_ < 8; ++i_) r_[i_] = qk4(Wq[i_], qf_); const bool d1_ = n & 1; \
      if ((n & 10) == 0) st128(v4u{d1_ ? pk(r_[4], r_[5]) : pk(r_[0], r_[1]), tag, d1_ ? pk(r_[6], r_[7]) : pk(r_[2], r_[3]), tag}, ws, \
                               (n >> 2) * 65536 + g * 32 + (n & 1) * 16, WS_QABS + hq * 2048 + chalf * 1024 + w * 128); } while (0)
    if (w != 0) {
      BAR();
      STAMP(1);
      LDQ(4, 4); LDQ(5, 1);
      asm volatile("" ::"v"(cpf));
      tag = __builtin_amdgcn_readfirstlane(ep) + 1u;
      if (q1) Q1PART(0, xa);
      LDQ(6, wink(0) - 1); LDQ(7, wink(1) - 1);
      const int g0 = (s * 7 + w - 1) * 1024;
      v4u K[24], V[24];
#define LDK(i) do { SB(); if (PACE) pace<(i), wink(i)>(tstart); else WAIT_VM(wink(i) - 1); K[i] = ld128<AUX_NT>(rk, voffK, g0 + (i) * (84 * 1024)); SB(); if (((i) & 1) == 0) TSTAMP(20 + (i) / 2); } while (0)
#define LDV(i) do { SB(); if (PACE) pace<24 + (i), winv(i)>(tstart); else WAIT_VM(winv(i) - 1); V[i] = ld128<AUX_NT>(rv, voffV, g0 + (i) * (84 * 1024)); SB(); if (((i) & 1) == 0) TSTAMP(32 + (i) / 2); } while (0)
      static_for<0, Q2SLOT>([&](auto ic) { constexpr int i = ic; LDK(i); });
      STAMP(10);
      if (q1) Q1PART(1, xb);
      bool todo = !q1;
      static_for<Q2SLOT, 24>([&](auto ic) {
        constexpr int i = ic;
        if (i <= Q2LAST && todo) {
          if (i == Q2LAST) lds_wait(L, LB_CNT + 12, 1u);
          if (i == Q2LAST || __builtin_amdgcn_readfirstlane(*(volatile LAS unsigned*)(L + LB_CNT + 12)) != 0u) {
            ORDER();
            const v4u qa = lds_ld<v4u>(L, LB_QA + lane * 16);
            Q2PART(qa);
            todo = false;
            STAMP(11);
          }
        }
        LDK(i);
      });
      LDV(0);
      STAMP(2);
      lds_wait(L, LB_CNT + 16, 1u);
      STAMP(3);
      const v4u qf = lds_ld<v4u>(L, LB_Q + lane * 16);
      float sc[24];
      static_for<0, 6>([&](auto jc) {
        constexpr int j = jc;
#pragma unroll
        for (int i = 4 * j; i < 4 * j + 4; ++i) sc[i] = qk4(K[i], qf);
        LDV(j + 1);
      });
      float m = sc[0];
#pragma unroll
      for (int i = 1; i < 24; ++i) m = fmaxf(m, sc[i]);
      m = rows_max(m) * SCL;
      LDV(7);
      float l = 0.f;
      v4u P[3];
      static_for<0, 3>([&](auto tc) {
        constexpr int t = tc;
#pragma unroll
        for (int i = 8 * t; i < 8 * t + 8; ++i) { sc[i] = ex2(__builtin_fmaf(sc[i], SCL, -m)); l += sc[i]; }
        P[t] = v4u{pk(sc[8 * t], sc[8 * t + 1]), pk(sc[8 * t + 2], sc[8 * t + 3]), pk(sc[8 * t + 4], sc[8 * t + 5]), pk(sc[8 * t + 6], sc[8 * t + 7])};
        LDV(8 + t);
      });
      l = rows_sum(l);
      if (lane < 16 && (n & 3) == 0) lds_st<f32x2>(L, LB_ML + w * 32 + (n >> 2) * 8, f32x2{m, l});
      lds_arrive(L, LB_CNT + 20, lane);
      STAMP(4);
      f32x4 acc[8];
#pragma unroll
      for (int j = 0; j < 8; ++j) acc[j] = f32x4{0.f, 0.f, 0.f, 0.f};
      static_for<11, 14>([&](auto ic) { constexpr int i = ic; LDV(i); });
      gemv8(P[0], V, acc);
      static_for<14, 22>([&](auto ic) { constexpr int i = ic; LDV(i); });
      gemv8(P[1], V + 8, acc);
      static_for<22, 24>([&](auto ic) { constexpr int i = ic; LDV(i); });
      STAMP(5);
      lds_arrive(L, LB_CNT + 24, lane);
      lds_wait(L, LB_CNT + 20, 8u);
      float M = -INFINITY, Ls = 0.f;
      f32x2 ml[8];
#pragma unroll
      for (int v = 0; v < 8; ++v) { ml[v] = lds_ld<f32x2>(L, LB_ML + v * 32 + g * 8); M = fmaxf(M, ml[v][0]); }
#pragma unroll
      for (int v = 0; v < 8; ++v) Ls = __builtin_fmaf(ex2(ml[v][0] - M), ml[v][1], Ls);
      const float mw = lds_ld<float>(L, LB_ML + w * 32 + g * 8);
      float f = ex2(mw - M) / Ls;
      asm volatile("" : "+v"(f));
      SB();
      gemv8(P[2], V + 16, acc);
      KEEP8(acc);
      lds_st<f32x4>(L, LB_O + w * 2048 + lane * 32, f32x4{acc[0][0] * f, acc[1][0] * f, acc[2][0] * f, acc[3][0] * f});
      lds_st<f32x4>(L, LB_O + w * 2048 + lane * 32 + 16, f32x4{acc[4][0] * f, acc[5][0] * f, acc[6][0] * f, acc[7][0] * f});
      STAMP(6);
      lds_arrive(L, LB_CNT, lane);
      if (w == 1 || w == 2) {
        lds_wait(L, LB_CNT, 8u);
        STAMP(7);
        const int pi = (w - 1) * 64 + lane;
        f32x4 o = lds_ld<f32x4>(L, LB_O + pi * 16);
#pragma unroll
        for (int v = 1; v < 8; ++v) o += lds_ld<f32x4>(L, LB_O + v * 2048 + pi * 16);
        st128(piece(o[0], o[1], o[2], o[3], tag), ws, (lane >> 5) * (24 * 512) + (lane & 31) * 16, WS_OB + (((4 * x + 2 * (w - 1)) * 2 + b) * 12 + s) * 512);
        STAMP(8);
      } else if (s < 4 && (w == 3 || w == 4)) {
        const int br = w == 3 ? 1 : 0, u = x * 8 + b * 4 + s;
        v4u f0, f1;
        _Pragma("nounroll") for (unsigned spin = 0; spin < SPIN_CAP; ++spin) {
          f0 = ld128<AUX_SC1>(ws, (lane >> 2) * 128 + (lane & 3) * 16, WS_FO + (br * 64 + u) * 4096);
          f1 = ld128<AUX_SC1>(ws, (lane >> 2) * 128 + (lane & 3) * 16, WS_FO + (br * 64 + u) * 4096 + 2048);
          if (all_ok(bad(f0, tag) | bad(f1, tag))) break;
          ORDER();
        }
        if (br) STAMP(12); else STAMP(13);
        float o[4] = {blo(f0[0]) + blo(f1[0]), bhi(f0[0]) + bhi(f1[0]), blo(f0[2]) + blo(f1[2]), bhi(f0[2]) + bhi(f1[2])};
#pragma unroll
        for (int e = 0; e < 4; ++e) { o[e] += dppf<ROR4>(o[e]); o[e] += dppf<ROR8>(o[e]); o[e] = rows_sum(o[e]); }
        if (lane < 4) {
          const int col = (u >> 5) * 1024 + br * 512 + ((u >> 3) & 3) * 128 + (((u >> 1) & 3) * 4 + lane) * 8 + (u & 1) * 4;
          *(GAS v2u*)((GAS char*)p.out + col * 2) = v2u{pk(o[0], o[1]), pk(o[2], o[3])};
        }
        if (u == 0 && br == 0 && lane == 0) st32(tag, ws, 0, WS_E);
        STAMP(9);
      }
      STAMP_FLUSH();
      asm volatile("s_endpgm" : "=v"(Wq[0]), "=v"(Wq[1]), "=v"(Wq[2]), "=v"(Wq[3]), "=v"(xa), "=v"(xb), "=v"(cpf), "=v"(ep)::"memory");
    }
    {
      if (lane < 2) lds_st<v4u>(L, LB_CNT + lane * 16, v4u{s < 4 || lane ? 0u : 1u, 0u, 0u, 0u});
      BAR();
      STAMP(1);
      LDQ(4, 5); LDQ(5, 5); LDQ(6, 5); LDQ(7, 5);
      asm volatile("" ::"v"(cpf));
      tag = __builtin_amdgcn_readfirstlane(ep) + 1u;
      if (q1) {
        Q1PART(0, xa);
        lds_wait(L, LB_CNT + 4, 8u);
        {
          f32x4 o = lds_ld<f32x4>(L, LB_R + lane * 16);
#pragma unroll
          for (int v = 1; v < 8; ++v) o += lds_ld<f32x4>(L, LB_R + v * 1024 + lane * 16);
          st128(piece(o[0], o[1], o[2], o[3], tag), ws, voffV, WS_G1A + (hq * 4 + mq) * 1024);
        }
        STAMP(2);
        Q1PART(1, xb);
        lds_wait(L, LB_CNT + 8, 8u);
        {
          f32x4 o = lds_ld<f32x4>(L, LB_R + 8192 + lane * 16);
#pragma unroll
          for (int v = 1; v < 8; ++v) o += lds_ld<f32x4>(L, LB_R + 8192 + v * 1024 + lane * 16);
          st128(piece(o[0], o[1], o[2], o[3], tag), ws, voffV, WS_G1B + (hq * 4 + mq) * 1024);
        }
        STAMP(3);
      } else {
        v4u qa[4];
        const int go = ((((n >> 2) & 1) * 2 + (n >> 3)) * 16 + 4 * g + (n & 3)) * 16;
        WAIT_VM(0);
        __builtin_amdgcn_s_sleep(SLP_QA);
        _Pragma("nounroll") for (unsigned spin = 0; spin < SPIN_CAP; ++spin) {
#pragma unroll
          for (int k = 0; k < 4; ++k) qa[k] = ld128<AUX_SC1>(ws, go, WS_G1A + (hq * 4 + k) * 1024);
          if (all_ok(bad(qa[0], tag) | bad(qa[1], tag) | bad(qa[2], tag) | bad(qa[3], tag))) break;
          ORDER();
        }
        STAMP(4);
        float s0 = 0.f, s1 = 0.f, s2 = 0.f, s3 = 0.f;
#pragma unroll
        for (int k = 0; k < 4; ++k) { s0 += blo(qa[k][0]); s1 += bhi(qa[k][0]); s2 += blo(qa[k][2]); s3 += bhi(qa[k][2]); }
        const unsigned o0 = pk(s0, s1), o1 = pk(s2, s3), p0 = dppu<ROR8>(o0), p1 = dppu<ROR8>(o1);
        const bool up = n >> 3;
        const v4u qf = {up ? p0 : o0, up ? p1 : o1, up ? o0 : p0, up ? o1 : p1};
        lds_st<v4u>(L, LB_QA + lane * 16, qf);
        lds_arrive(L, LB_CNT + 12, lane);
        Q2PART(qf);
        STAMP(5);
      }
      v4u qb[8];
      {
        const int go = (n >> 2) * 4096 + (4 * g + (n & 3)) * 16;
        __builtin_amdgcn_s_sleep(SLP_QB);
        _Pragma("nounroll") for (unsigned spin = 0; spin < SPIN_CAP; ++spin) {
          unsigned bb = 0;
#pragma unroll
          for (int k = 0; k < 8; ++k) qb[k] = ld128<AUX_SC1>(ws, go, WS_G1B + x * 16384 + (k >> 1) * 1024 + (b * 2 + (k & 1)) * 256);
#pragma unroll
          for (int k = 0; k < 8; ++k) bb |= bad(qb[k], tag);
          if (all_ok(bb)) break;
          __builtin_amdgcn_s_sleep(16);
          ORDER();
        }
      }
      float q8[8];
#pragma unroll
      for (int e = 0; e < 8; ++e) q8[e] = 0.f;
#pragma unroll
      for (int k = 0; k < 8; ++k) { const int e = (k & 1) * 4; q8[e] += blo(qb[k][0]); q8[e + 1] += bhi(qb[k][0]); q8[e + 2] += blo(qb[k][2]); q8[e + 3] += bhi(qb[k][2]); }
      const v4u qf = {pk(q8[0], q8[1]), pk(q8[2], q8[3]), pk(q8[4], q8[5]), pk(q8[6], q8[7])};
      lds_st<v4u>(L, LB_Q + lane * 16, qf);
      lds_arrive(L, LB_CNT + 16, lane);
      STAMP(6);
      f32x4 acc[8];
#pragma unroll
      for (int j = 0; j < 8; ++j) acc[j] = f32x4{0.f, 0.f, 0.f, 0.f};
      if (s < 4) {
        const int g0 = (2016 + s * 8) * 1024;
        v4u K[8], V[8];
#pragma unroll
        for (int i = 0; i < 8; ++i) { WAIT_VM(2); K[i] = ld128<AUX_NT>(rk, voffK, g0 + i * 1024); SB(); }
#pragma unroll
        for (int i = 0; i < 8; ++i) { WAIT_VM(2); V[i] = ld128<AUX_NT>(rv, voffV, g0 + i * 1024); SB(); }
        float sc[8];
#pragma unroll
        for (int i = 0; i < 8; ++i) sc[i] = qk4(K[i], qf);
        float m = sc[0];
#pragma unroll
        for (int i = 1; i < 8; ++i) m = fmaxf(m, sc[i]);
        m = rows_max(m) * SCL;
        float l = 0.f;
#pragma unroll
        for (int i = 0; i < 8; ++i) { sc[i] = ex2(__builtin_fmaf(sc[i], SCL, -m)); l += sc[i]; }
        const v4u P = {pk(sc[0], sc[1]), pk(sc[2], sc[3]), pk(sc[4], sc[5]), pk(sc[6], sc[7])};
        l = rows_sum(l);
        if (lane < 16 && (n & 3) == 0) lds_st<f32x2>(L, LB_ML + (n >> 2) * 8, f32x2{m, l});
        gemv8(P, V, acc);
        KEEP8(acc);
      } else {
        if (lane < 4) lds_st<f32x2>(L, LB_ML + lane * 8, f32x2{-INFINITY, 0.f});
        lds_st<f32x4>(L, LB_O + lane * 32, f32x4{0.f, 0.f, 0.f, 0.f});
        lds_st<f32x4>(L, LB_O + lane * 32 + 16, f32x4{0.f, 0.f, 0.f, 0.f});
      }
      lds_arrive(L, LB_CNT + 20, lane);
      lds_wait<4>(L, LB_CNT + 20, 8u);
      float M = -INFINITY, Ls = 0.f;
      f32x2 ml[8];
#pragma unroll
      for (int v = 0; v < 8; ++v) { ml[v] = lds_ld<f32x2>(L, LB_ML + v * 32 + g * 8); M = fmaxf(M, ml[v][0]); }
#pragma unroll
      for (int v = 0; v < 8; ++v) Ls = __builtin_fmaf(ex2(ml[v][0] - M), ml[v][1], Ls);
      if (n == 0) st64(v2u{asu(M + __builtin_amdgcn_logf(Ls)), tag}, ws, g * (24 * 128), WS_LB + ((4 * x * 2 + b) * 12 + s) * 128);
      SB();
      if (s < 4) {
        const float f = ex2(ml[0][0] - M) / Ls;
        lds_st<f32x4>(L, LB_O + lane * 32, f32x4{acc[0][0] * f, acc[1][0] * f, acc[2][0] * f, acc[3][0] * f});
        lds_st<f32x4>(L, LB_O + lane * 32 + 16, f32x4{acc[4][0] * f, acc[5][0] * f, acc[6][0] * f, acc[7][0] * f});
        lds_arrive(L, LB_CNT, lane);
      } else {
        const int idx = b * 8 + s - 4, h = 4 * (x ^ 4) + (idx >> 2), cb = idx & 3;
        const rsrc_t rwo = mk_rsrc(p.woA, 4 << 20);
        lds_wait<2>(L, LB_CNT + 24, NTRIG);
        STAMP(7);
        v4u ca[8], W[32], T[32];
        const int go = ((((n >> 2) & 1) * 2 + (n >> 3)) * 16 + 4 * (n & 3) + g) * 16;
#pragma unroll
        for (int i = 0; i < 32; ++i) { SB(); WAIT_VM(WIN_H - 1); W[i] = ld128<AUX_NT>(rwo, voffW1, (h * 128 + (i >> 3) * 32 + (i & 7)) * 1024 + cb * 256); }
        SB();
#pragma unroll
        for (int k = 0; k < 8; ++k) ca[k] = ld128<AUX_SC1>(ws, go, WS_CA + (h * 8 + k) * 1024);
        SB();
#pragma unroll
        for (int kq = 0; kq < 4; ++kq) { v4u* Tk = T + 8 * kq; tr8(W + 8 * kq, Tk); KEEP8(Tk); SB(); }
        STAMP(10);
        _Pragma("nounroll") for (unsigned spin = 0; spin < SPIN_CAP; ++spin) {
          unsigned bb = 0;
#pragma unroll
          for (int k = 0; k < 8; ++k) bb |= bad(ca[k], tag);
          if (all_ok(bb)) break;
          ORDER();
#pragma unroll
          for (int k = 0; k < 8; ++k) ca[k] = ld128<AUX_SC1>(ws, go, WS_CA + (h * 8 + k) * 1024);
        }
        STAMP(8);
        float s0 = 0.f, s1 = 0.f, s2 = 0.f, s3 = 0.f;
#pragma unroll
        for (int k = 0; k < 8; ++k) { s0 += blo(ca[k][0]); s1 += bhi(ca[k][0]); s2 += blo(ca[k][2]); s3 += bhi(ca[k][2]); }
        const unsigned o0 = pk(s0, s1), o1 = pk(s2, s3), p0 = dppu<ROR8>(o0), p1 = dppu<ROR8>(o1);
        const bool up = n >> 3;
        const v4u cf = {up ? p0 : o0, up ? p1 : o1, up ? o0 : p0, up ? o1 : p1};
#pragma unroll
        for (int kq = 0; kq < 4; ++kq) {
          const bool on = (n & 3) == kq;
          const v4u am = {on ? cf[0] : 0u, on ? cf[1] : 0u, on ? cf[2] : 0u, on ? cf[3] : 0u};
#pragma unroll
          for (int j = 0; j < 8; ++j) acc[j] = mfma16(am, T[8 * kq + j], acc[j]);
        }
        float o[8];
#pragma unroll
        for (int j = 0; j < 8; ++j) o[j] = (acc[j][0] + acc[j][1]) + (acc[j][2] + acc[j][3]);
        const bool hi = g >> 1;
        st128(piece(hi ? o[4] : o[0], hi ? o[5] : o[1], hi ? o[6] : o[2], hi ? o[7] : o[3], tag), ws,
              ((g & 1) * 32 + (n >> 2) * 2 + (g >> 1)) * 4096 + (n & 3) * 16, WS_FO + cb * 8 * 4096 + h * 128);
        STAMP(9);
      }
      STAMP_FLUSH();
    }
  }
}

}

extern "C" void kernel_launch(void* const* d_in, const int* in_sizes, int n_in, void* d_out, int out_size, void* d_ws, size_t ws_size,
                              hipStream_t stream) {
  Params p;
  p.xA = d_in[0]; p.ckv = d_in[1]; p.wqA = d_in[2]; p.wuk = d_in[3]; p.wuv = d_in[4]; p.woA = d_in[5];
  p.xB = d_in[6]; p.kc = d_in[7]; p.vc = d_in[8]; p.wqB = d_in[9]; p.woB = d_in[10];
  p.out = d_out; p.ws = d_ws;
  fused_decode<<<256, 512, 0, stream>>>(p);
}
```
